```python
import torch

def _softmax(x, axis=-1):
    return torch.softmax(x, dim=axis)
def _gelu(x):
    return torch.nn.functional.gelu(x, approximate="tanh")
def _silu(x):
    return torch.nn.functional.silu(x)
def _sigmoid(x):
    return torch.sigmoid(x)
def _layernorm(x, axis=-1, eps=1e-5):
    m = x.mean(dim=axis, keepdim=True)
    v = x.var(dim=axis, keepdim=True, unbiased=False)
    return (x - m) / torch.sqrt(v + eps)
def _scatter_add(tgt, idx, src, axis=0):
    return tgt.clone().index_add_(axis, idx, src)
def _segment_sum(src, seg, n):
    out = torch.zeros((n,) + src.shape[1:], dtype=src.dtype)
    return out.index_add_(0, seg, src)
def _pad(x, pw):
    flat = tuple(v for pair in reversed(pw) for v in pair)
    return torch.nn.functional.pad(x, flat)

kernel_name = 'recurrent_scan__ssm_conv_s4d__moe_arch_mod__prompt'

def setup_inputs():

    _w = (lambda a: (lambda t: t.to(torch.bfloat16) if t.is_floating_point() else t)(torch.as_tensor(a)))
    def _A_setup():
        Bsz, L, D, N = 1, 2048, 512, 128
        return {
            "u": _w((lambda *s: torch.randn(*s, dtype=torch.bfloat16) * 0.05)(Bsz, L, D)),
            "log_A_real": _w((lambda *s: torch.randn(*s, dtype=torch.bfloat16) * 3.2)(D, N) * 0.5),
            "A_imag": _w((lambda *s: torch.randn(*s, dtype=torch.bfloat16) * 0.05)(D, N) * 3.14),
            "C_real": _w((lambda *s: torch.randn(*s, dtype=torch.bfloat16) * 0.1)(D, N) / N ** 0.5),
            "C_imag": _w((lambda *s: torch.randn(*s, dtype=torch.bfloat16) * 0.1)(D, N) / N ** 0.5),
            "log_dt": _w((lambda *s: torch.randn(*s, dtype=torch.bfloat16) * 0.05)(D) * 0.5 - 4.0),
            "D_skip": _w((lambda *s: torch.randn(*s, dtype=torch.bfloat16) * 0.05)(D)),
        }
    _A_in = _A_setup()

    def _B_setup():
        B, S, D, F, C = 1, 2048, 512, 1376, 2
        return {
            "x": _w((lambda *s: torch.randn(*s, dtype=torch.bfloat16) * 0.05)(B, S, D)),
            "w_r": _w((lambda *s: torch.randn(*s, dtype=torch.bfloat16) * 3.2)(D)),
            "w_gate": _w((lambda *s: torch.randn(*s, dtype=torch.bfloat16) * 0.1)(D, F)),
            "w_up": _w((lambda *s: torch.randn(*s, dtype=torch.bfloat16) * 0.1)(D, F)),
            "w_down": _w((lambda *s: torch.randn(*s, dtype=torch.bfloat16) * 0.1)(F, D)),
        }
    _B_in = _B_setup()

    return {
        "A_u": _A_in["u"],
        "A_log_A_real": _A_in["log_A_real"],
        "A_A_imag": _A_in["A_imag"],
        "A_C_real": _A_in["C_real"],
        "A_C_imag": _A_in["C_imag"],
        "A_log_dt": _A_in["log_dt"],
        "A_D_skip": _A_in["D_skip"],
        "B_x": _B_in["x"],
        "B_w_r": _B_in["w_r"],
        "B_w_gate": _B_in["w_gate"],
        "B_w_up": _B_in["w_up"],
        "B_w_down": _B_in["w_down"],
    }

def reference(A_u, A_log_A_real, A_A_imag, A_C_real, A_C_imag, A_log_dt, A_D_skip, B_x, B_w_r, B_w_gate, B_w_up, B_w_down):
    def _A_ref(u, log_A_real, A_imag, C_real, C_imag, log_dt, D_skip):
        L = u.shape[1]
        dt = torch.exp(log_dt)
        A = -torch.exp(log_A_real) + 1j * A_imag
        C = (C_real + 1j * C_imag) * dt[:, None]
        pos = (lambda x, dt: x.to(dt))(torch.arange(L), log_dt.dtype)
        P = torch.exp((A * dt[:, None])[:, :, None] * pos[None, None, :])
        K = 2.0 * torch.einsum("dn,dnl->dl", C, P).real
        L2 = 2 * L
        u_t = (lambda x, *d: x.permute(*d))(u, 0, 2, 1)
        Kf = (lambda x, *a, **k: torch.fft.rfft(x.float(), *a, **k))(K, L2)
        Uf = (lambda x, *a, **k: torch.fft.rfft(x.float(), *a, **k))(u_t, L2)
        y = (lambda x, *a, **k: torch.fft.irfft(x, *a, **k).to(torch.bfloat16))(Uf * Kf[None, :, :], L2)[:, :, :L]
        y = (lambda x, *d: x.permute(*d))(y, 0, 2, 1)
        return y + u * D_skip[None, None, :]

    def _B_ref(x, w_r, w_gate, w_up, w_down):
        B, S, D = x.shape
        C = 2
        r = x @ w_r
        idx = torch.argsort(-r, axis=-1)[:, :C]
        idx_e = (lambda x, n, axis: torch.repeat_interleave(x, n, dim=axis))(idx.reshape(B, C, 1), D, axis=2)
        x_sel = (lambda x, i, axis: torch.take_along_dim(x, i, dim=axis))(x, idx_e, axis=1)
        g = _silu(x_sel @ w_gate)
        u = x_sel @ w_up
        y = (g * u) @ w_down
        r_sel = _sigmoid((lambda x, i, axis: torch.take_along_dim(x, i, dim=axis))(r, idx, axis=1))
        y = y * r_sel.reshape(B, C, 1)
        b_off = (torch.arange(B) * S).reshape(B, 1)
        flat_idx = (idx + b_off).reshape(B * C)
        zero = (x * 0.0).reshape(B * S, D)
        upd = _scatter_add(zero, flat_idx, y.reshape(B * C, D), axis=0)
        return x + upd.reshape(B, S, D)

    x = A_u
    xn = _layernorm(x, axis=-1)
    s = _A_ref(xn, A_log_A_real, A_A_imag, A_C_real, A_C_imag, A_log_dt, A_D_skip)
    g = _silu(_B_ref(xn, B_w_r, B_w_gate, B_w_up, B_w_down))
    return x + s * g
```

```cpp
#include <hip/hip_runtime.h>
#include <hip/hip_bf16.h>
#include <stdint.h>

typedef unsigned short u16;
constexpr int L = 2048, D = 512, N = 128, F = 1376;

__device__ __forceinline__ float bf2f(u16 v) { return __uint_as_float(((unsigned)v) << 16); }
__device__ __forceinline__ u16 f2bf(float f) {
    __hip_bfloat16 b = __float2bfloat16(f);
    return *reinterpret_cast<u16*>(&b);
}
__device__ __forceinline__ float rbf(float f) { return bf2f(f2bf(f)); }
__device__ __forceinline__ float silu_f(float x) { return x / (1.f + expf(-x)); }

__global__ void k_ln(const u16* x, const u16* wr, u16* xn, float* r) {
    int t = blockIdx.x, lane = threadIdx.x;
    float v[8];
    float s = 0;
    for (int i = 0; i < 8; i++) { v[i] = bf2f(x[t * D + lane * 8 + i]); s += v[i]; }
    for (int m = 32; m >= 1; m >>= 1) s += __shfl_xor(s, m);
    float mean = s / D;
    float q = 0;
    for (int i = 0; i < 8; i++) { float d = v[i] - mean; q += d * d; }
    for (int m = 32; m >= 1; m >>= 1) q += __shfl_xor(q, m);
    float var = rbf(q / D);
    float mb = rbf(mean);
    float sd = rbf(sqrtf(rbf(var + 1e-5f)));
    float acc = 0;
    for (int i = 0; i < 8; i++) {
        float a = rbf(rbf(v[i] - mb) / sd);
        xn[t * D + lane * 8 + i] = f2bf(a);
        acc += a * bf2f(wr[lane * 8 + i]);
    }
    for (int m = 32; m >= 1; m >>= 1) acc += __shfl_xor(acc, m);
    if (lane == 0) r[t] = rbf(acc);
}

__global__ void k_kgen(const u16* logA, const u16* Aim, const u16* Cre, const u16* Cim, const u16* logdt, float* K) {
    int d = blockIdx.x;
    float dt = rbf(expf(bf2f(logdt[d])));
    for (int l = threadIdx.x; l < L; l += blockDim.x) {
        float p = rbf((float)l);
        float acc = 0;
        for (int n = 0; n < N; n++) {
            float ar = -rbf(expf(bf2f(logA[d * N + n]))) * dt;
            float ai = bf2f(Aim[d * N + n]) * dt;
            float cr = bf2f(Cre[d * N + n]) * dt, ci = bf2f(Cim[d * N + n]) * dt;
            float e = expf(ar * p);
            float sn, cs;
            sincosf(ai * p, &sn, &cs);
            acc += e * (cr * cs - ci * sn);
        }
        K[d * L + l] = 2.f * acc;
    }
}

__global__ void k_top2(const float* r, int* idx) {
    if (threadIdx.x == 0) {
        int b0 = 0; float v0 = r[0];
        for (int i = 1; i < L; i++) if (r[i] > v0) { v0 = r[i]; b0 = i; }
        int b1 = (b0 == 0) ? 1 : 0; float v1 = r[b1];
        for (int i = 0; i < L; i++) if (i != b0 && r[i] > v1) { v1 = r[i]; b1 = i; }
        idx[0] = b0; idx[1] = b1;
    }
}

__global__ void k_gu(const u16* xn, const int* idx, const u16* wg, const u16* wu, float* h) {
    int f = blockIdx.x * blockDim.x + threadIdx.x;
    if (f >= F) return;
    for (int c = 0; c < 2; c++) {
        const u16* xr = xn + idx[c] * D;
        float g = 0, u = 0;
        for (int d = 0; d < D; d++) { float a = bf2f(xr[d]); g += a * bf2f(wg[d * F + f]); u += a * bf2f(wu[d * F + f]); }
        g = rbf(silu_f(rbf(g))); u = rbf(u);
        h[c * F + f] = rbf(g * u);
    }
}

__global__ void k_down(const float* h, const float* r, const int* idx, const u16* wd, float* y) {
    int d = blockIdx.x * blockDim.x + threadIdx.x;
    for (int c = 0; c < 2; c++) {
        float a = 0;
        for (int f = 0; f < F; f++) a += h[c * F + f] * bf2f(wd[f * D + d]);
        float rs = rbf(1.f / (1.f + expf(-r[idx[c]])));
        y[c * D + d] = rbf(rbf(a) * rs);
    }
}

__global__ void k_conv(const u16* x, const u16* xn, const float* K, const u16* dskip, const int* idx, const float* y, u16* out) {
    __shared__ float su[L];
    __shared__ float sk[L];
    int d = blockIdx.x;
    for (int l = threadIdx.x; l < L; l += blockDim.x) { su[l] = bf2f(xn[l * D + d]); sk[l] = K[d * L + l]; }
    __syncthreads();
    float ds = bf2f(dskip[d]);
    for (int l = threadIdx.x; l < L; l += blockDim.x) {
        float a = 0;
        for (int j = 0; j <= l; j++) a += sk[j] * su[l - j];
        float u = su[l];
        float s = rbf(rbf(a) + rbf(u * ds));
        float b = u;
        if (l == idx[0]) b = rbf(u + y[d]);
        if (l == idx[1]) b = rbf(u + y[D + d]);
        float g = rbf(silu_f(b));
        out[l * D + d] = f2bf(bf2f(x[l * D + d]) + rbf(s * g));
    }
}

extern "C" void kernel_launch(void* const* d_in, const int* in_sizes, int n_in,
                              void* d_out, int out_size, void* d_ws, size_t ws_size,
                              hipStream_t stream) {
    const u16* x = (const u16*)d_in[0];
    const u16* logA = (const u16*)d_in[1];
    const u16* Aim = (const u16*)d_in[2];
    const u16* Cre = (const u16*)d_in[3];
    const u16* Cim = (const u16*)d_in[4];
    const u16* logdt = (const u16*)d_in[5];
    const u16* dskip = (const u16*)d_in[6];
    const u16* wr = (const u16*)d_in[8];
    const u16* wg = (const u16*)d_in[9];
    const u16* wu = (const u16*)d_in[10];
    const u16* wd = (const u16*)d_in[11];
    char* ws = (char*)d_ws;
    u16* xn = (u16*)ws;
    float* K = (float*)(ws + (2 << 20));
    float* r = (float*)(ws + (6 << 20));
    int* idx = (int*)(ws + (6 << 20) + 16384);
    float* h = (float*)(ws + (6 << 20) + 32768);
    float* y = (float*)(ws + (6 << 20) + 65536);
    k_ln<<<L, 64, 0, stream>>>(x, wr, xn, r);
    k_kgen<<<D, 256, 0, stream>>>(logA, Aim, Cre, Cim, logdt, K);
    k_top2<<<1, 64, 0, stream>>>(r, idx);
    k_gu<<<(F + 63) / 64, 64, 0, stream>>>(xn, idx, wg, wu, h);
    k_down<<<D / 64, 64, 0, stream>>>(h, r, idx, wd, y);
    k_conv<<<D, 256, 0, stream>>>(x, xn, K, dskip, idx, y, (u16*)d_out);
}
```

```cpp
#include <hip/hip_runtime.h>
#include <stdint.h>

typedef unsigned short u16;
typedef unsigned u32;
typedef unsigned long long u64;
typedef _Float16 h8 __attribute__((ext_vector_type(8)));
typedef _Float16 h2 __attribute__((ext_vector_type(2)));
typedef __bf16 b8 __attribute__((ext_vector_type(8)));
typedef __bf16 b2 __attribute__((ext_vector_type(2)));
typedef short s4 __attribute__((ext_vector_type(4)));
typedef short s2 __attribute__((ext_vector_type(2)));
typedef float f2 __attribute__((ext_vector_type(2)));
typedef float f4 __attribute__((ext_vector_type(4)));
typedef float f16v __attribute__((ext_vector_type(16)));
typedef u32 u2 __attribute__((ext_vector_type(2)));
typedef u32 u4 __attribute__((ext_vector_type(4)));
#define GAS __attribute__((address_space(1)))
#define LAS __attribute__((address_space(3)))
typedef GAS u64 gu64;
typedef GAS u32 gu32;
#define RLX_AGENT __ATOMIC_RELAXED, __HIP_MEMORY_SCOPE_AGENT

constexpr int L = 2048, D = 512, NM = 128, F = 1376;
constexpr int NFFN = 43;
constexpr int WS_STATS = 4096;
constexpr int WS_KEYS = WS_STATS + L * 8;
constexpr int WS_ACKS = WS_KEYS + 128 * 128;
constexpr int WS_PARTS = WS_ACKS + 128 * 128;
constexpr int WS_STAMP = 1 << 20;
constexpr int KROW = 528;
constexpr int KCH = 53 * KROW;
constexpr int O_KOP = 0;
constexpr int O_Y = 0;
constexpr int YCH = 2560 * 4;
constexpr int O_U = 4 * KCH;
constexpr int UCH = 2304 * 2;
constexpr int O_K = O_U + 4 * UCH;
constexpr int KA = 3840;
constexpr int O_MISC = O_K + 4 * KA;
constexpr int LDS_BYTES = O_MISC + 256;
constexpr u32 SPIN_CAP = 1u << 20;

#define STAMP(k)

__device__ __forceinline__ float bflo(u32 v) { return __uint_as_float(v << 16); }
__device__ __forceinline__ float bfhi(u32 v) { return __uint_as_float(v & 0xffff0000u); }
__device__ __forceinline__ u32 pkbf(float a, float b) { b2 r = {(__bf16)a, (__bf16)b}; return __builtin_bit_cast(u32, r); }
__device__ __forceinline__ float rbf(float a) { return bflo(pkbf(a, a)); }
__device__ __forceinline__ u32 pkh(float a, float b) { h2 r = {(_Float16)a, (_Float16)b}; return __builtin_bit_cast(u32, r); }
__device__ __forceinline__ float ex2(float x) { return __builtin_amdgcn_exp2f(x); }
__device__ __forceinline__ float rcp(float x) { return __builtin_amdgcn_rcpf(x); }
constexpr float LOG2E = 1.4426950408889634f;
__device__ __forceinline__ float sigm(float x) { return rcp(1.f + ex2(-LOG2E * x)); }
__device__ __forceinline__ void bar() { asm volatile("s_waitcnt lgkmcnt(0)\n\ts_barrier" ::: "memory"); }
__device__ __forceinline__ float dot2bf(u32 a, u32 b, float c) {
    return __builtin_amdgcn_fdot2_f32_bf16(__builtin_bit_cast(b2, a), __builtin_bit_cast(b2, b), c, false);
}
template <int CTRL> __device__ __forceinline__ float dppf(float v) {
    return __int_as_float(__builtin_amdgcn_update_dpp(0, __float_as_int(v), CTRL, 0xf, 0xf, true));
}
template <int CTRL> __device__ __forceinline__ u32 dppu(u32 v) { return (u32)__builtin_amdgcn_update_dpp(0, (int)v, CTRL, 0xf, 0xf, true); }
__device__ __forceinline__ float rowsum(float v) {
    v += dppf<0x128>(v); v += dppf<0x124>(v); v += dppf<0x122>(v); v += dppf<0x121>(v); return v;
}
__device__ __forceinline__ u32 rowmax(u32 v) {
    v = max(v, dppu<0x128>(v)); v = max(v, dppu<0x124>(v)); v = max(v, dppu<0x122>(v)); v = max(v, dppu<0x121>(v)); return v;
}
__device__ __forceinline__ u32 wavemax(u32 v) {
    v = rowmax(v);
    u32 a = __builtin_amdgcn_readlane(v, 0), b = __builtin_amdgcn_readlane(v, 16), c = __builtin_amdgcn_readlane(v, 32), d = __builtin_amdgcn_readlane(v, 48);
    return max(max(a, b), max(c, d));
}
__device__ __forceinline__ u64 gran(u32 tag, u32 v) { return ((u64)tag << 32) | v; }
__device__ __forceinline__ f2 cmul(f2 p, f2 w, f2 wr) { return p.xx * w + p.yy * wr; }
__device__ __forceinline__ f2 rot(f2 w) { return (f2){-w.y, w.x}; }
__device__ __forceinline__ f2 cexp(float x2, float th, float p) {
    float m = ex2(x2 * p), a = __builtin_amdgcn_fractf(th * p);
    return (f2){m * __builtin_amdgcn_cosf(a), m * __builtin_amdgcn_sinf(a)};
}
__device__ __forceinline__ f4 mfma16(u4 a, u4 b, f4 c) {
    return __builtin_amdgcn_mfma_f32_16x16x32_f16(__builtin_bit_cast(h8, a), __builtin_bit_cast(h8, b), c, 0, 0, 0);
}
__device__ __forceinline__ f4 mfma16b(u4 a, u4 b, f4 c) {
    return __builtin_amdgcn_mfma_f32_16x16x32_bf16(__builtin_bit_cast(b8, a), __builtin_bit_cast(b8, b), c, 0, 0, 0);
}
__device__ __forceinline__ void gather_top2(char* ws, u32 tag, int lane, u32& k1, u32& k2) {
    gu64* kp = (gu64*)(ws + WS_KEYS) + (lane >> 1) * 16 + (lane & 1);
    u64 v[4];
    for (u32 spins = 0; spins < SPIN_CAP; ++spins) {
        bool ok = true;
#pragma unroll
        for (int k = 0; k < 4; k++) { v[k] = __hip_atomic_load(kp + k * 512, RLX_AGENT); ok &= (u32)(v[k] >> 32) == tag; }
        if (ok) break;
        __builtin_amdgcn_s_sleep(1);
    }
    u32 a = (u32)v[0], b = (u32)v[1], c = (u32)v[2], d = (u32)v[3];
    k1 = wavemax(max(max(a, b), max(c, d)));
    a = a == k1 ? 0 : a; b = b == k1 ? 0 : b; c = c == k1 ? 0 : c; d = d == k1 ? 0 : d;
    k2 = wavemax(max(max(a, b), max(c, d)));
}
__device__ __forceinline__ int key_tok(u32 k) { return 2047 - (int)(k & 2047u); }
__device__ __forceinline__ float key_r(u32 k) { u32 m = k >> 16; m = (m & 0x8000u) ? (m ^ 0x8000u) : (~m & 0xffffu); return bflo(m); }

__global__ void __launch_bounds__(512, 2) s4d_mod_kernel(const u16* __restrict__ x, const u16* __restrict__ logA, const u16* __restrict__ Aim,
        const u16* __restrict__ Cre, const u16* __restrict__ Cim, const u16* __restrict__ logdt, const u16* __restrict__ dskip,
        const u16* __restrict__ wr, const u16* __restrict__ wg, const u16* __restrict__ wu, const u16* __restrict__ wd,
        u16* __restrict__ out, char* ws) {
    __shared__ __attribute__((aligned(16))) char smem[LDS_BYTES];
    LAS char* lds = (LAS char*)smem;
    const int tid = threadIdx.x, lane = tid & 63;
    const int w = __builtin_amdgcn_readfirstlane(tid >> 6);
    const int bx = blockIdx.x, xg = bx & 7, jj = bx >> 3;
    const int idx = xg * 16 + (jj >> 1);
    u32 tag;
    STAMP(0);
    if (jj & 1) {
        const int tok = idx * 16 + 2 * w + (lane >> 5), l32 = lane & 31;
        const u4 x0 = *(const u4*)(x + tok * D + l32 * 8), x1 = *(const u4*)(x + tok * D + 256 + l32 * 8);
        const u4 w0 = *(const u4*)(wr + l32 * 8), w1 = *(const u4*)(wr + 256 + l32 * 8);
        asm volatile("s_load_dword %0, %1, 0\n\ts_waitcnt lgkmcnt(0)" : "=s"(tag) : "s"(ws) : "memory");
        tag += 1;
        float S = 0, Q = 0, R = 0, W = 0;
#pragma unroll
        for (int k = 0; k < 4; k++) {
            S = dot2bf(x0[k], 0x3f803f80u, S); Q = dot2bf(x0[k], x0[k], Q); R = dot2bf(x0[k], w0[k], R); W = dot2bf(w0[k], 0x3f803f80u, W);
            S = dot2bf(x1[k], 0x3f803f80u, S); Q = dot2bf(x1[k], x1[k], Q); R = dot2bf(x1[k], w1[k], R); W = dot2bf(w1[k], 0x3f803f80u, W);
        }
        STAMP(1);
        {
            auto p = __builtin_amdgcn_permlane16_swap(__float_as_uint(S), __float_as_uint(Q), false, false);
            auto q = __builtin_amdgcn_permlane16_swap(__float_as_uint(R), __float_as_uint(W), false, false);
            float a = rowsum(__uint_as_float(p[0]) + __uint_as_float(p[1]));
            float b = rowsum(__uint_as_float(q[0]) + __uint_as_float(q[1]));
            auto p2 = __builtin_amdgcn_permlane16_swap(__float_as_uint(a), __float_as_uint(a), false, false);
            auto q2 = __builtin_amdgcn_permlane16_swap(__float_as_uint(b), __float_as_uint(b), false, false);
            S = __uint_as_float(p2[0]); Q = __uint_as_float(p2[1]); R = __uint_as_float(q2[0]); W = __uint_as_float(q2[1]);
        }
        const float mean = S * (1.f / D);
        const float var = rbf(fmaxf(Q * (1.f / D) - mean * mean, 0.f));
        const float sd = __builtin_amdgcn_sqrtf(rbf(var + 1e-5f));
        const u32 st = pkbf(sd, mean);
        const float r = (R - bfhi(st) * W) * rcp(bflo(st));
        u32 rb = pkbf(r, r) & 0xffffu;
        rb = (rb & 0x8000u) ? (~rb & 0xffffu) : (rb | 0x8000u);
        const u32 key = (rb << 16) | (u32)(2047 - tok);
        LAS u32* sm = (LAS u32*)(lds + O_MISC);
        if (l32 == 0) { sm[tok & 15] = st; sm[16 + (tok & 15)] = key; }
        bar();
        if (w == 0) {
            const u32 s_ = sm[lane & 15], k_ = sm[16 + (lane & 15)];
            if (lane < 16) __hip_atomic_store((gu64*)(ws + WS_STATS) + idx * 16 + lane, gran(tag, s_), RLX_AGENT);
            const u32 m1 = rowmax(k_), m2 = rowmax(k_ == m1 ? 0u : k_);
            if (lane < 2) __hip_atomic_store((gu64*)(ws + WS_KEYS) + idx * 16 + lane, gran(tag, lane ? m2 : m1), RLX_AGENT);
        }
        STAMP(2);
        const int fi = (jj >> 1) * 8 + xg;
        if (fi == 127) {
            if (w == 0) {
                u32 k1, k2;
                gather_top2(ws, tag, lane, k1, k2);
                gu64* ap = (gu64*)(ws + WS_ACKS) + lane * 16;
                for (u32 spins = 0; spins < SPIN_CAP; ++spins) {
                    const u64 a = __hip_atomic_load(ap, RLX_AGENT), b = __hip_atomic_load(ap + 1024, RLX_AGENT);
                    if ((u32)(a >> 32) == tag && (u32)(b >> 32) == tag) break;
                    __builtin_amdgcn_s_sleep(4);
                }
                asm volatile("" :: "s"(k1), "s"(k2));
                if (lane == 0) __hip_atomic_store((gu32*)ws, tag, RLX_AGENT);
            }
            return;
        }
        if (fi >= 48) return;
        const int fp = xg * 6 + (jj >> 1);
        if (fp >= NFFN) return;
        const int f0 = fp * 32;
        LAS char* wl = lds + w * 12288;
        u4 t[12];
#pragma unroll
        for (int k = 0; k < 4; k++) {
            const int d = 64 * w + 16 * k + (lane >> 2);
            t[k] = *(const u4*)(wg + d * F + f0 + (lane & 3) * 8);
            t[4 + k] = *(const u4*)(wu + d * F + f0 + (lane & 3) * 8);
            t[8 + k] = *(const u4*)(wd + (f0 + 8 * k + (lane >> 3)) * D + 64 * w + (lane & 7) * 8);
        }
#pragma unroll
        for (int k = 0; k < 12; k++) *(LAS u4*)(wl + k * 1024 + lane * 16) = t[k];
        u4 bg[2][2][2], bd[4];
        {
            const int g = lane >> 4, q = (lane >> 2) & 3, p = lane & 3;
#pragma unroll
            for (int m = 0; m < 2; m++)
#pragma unroll
                for (int s = 0; s < 2; s++)
#pragma unroll
                    for (int c = 0; c < 2; c++) {
                        LAS char* a = wl + m * 4096 + (32 * s + 8 * g + q) * 64 + c * 32 + p * 8;
                        s4 lo = __builtin_amdgcn_ds_read_tr16_b64_v4i16((LAS s4*)a), hi = __builtin_amdgcn_ds_read_tr16_b64_v4i16((LAS s4*)(a + 256));
                        u2 l2 = __builtin_bit_cast(u2, lo), h2_ = __builtin_bit_cast(u2, hi);
                        bg[m][s][c] = (u4){l2.x, l2.y, h2_.x, h2_.y};
                    }
#pragma unroll
            for (int c = 0; c < 4; c++) {
                LAS char* a = wl + 8192 + (8 * g + q) * 128 + c * 32 + p * 8;
                s4 lo = __builtin_amdgcn_ds_read_tr16_b64_v4i16((LAS s4*)a), hi = __builtin_amdgcn_ds_read_tr16_b64_v4i16((LAS s4*)(a + 512));
                u2 l2 = __builtin_bit_cast(u2, lo), h2_ = __builtin_bit_cast(u2, hi);
                bd[c] = (u4){l2.x, l2.y, h2_.x, h2_.y};
            }
        }
        STAMP(3);
        if (w == 0) {
            u32 k1, k2;
            gather_top2(ws, tag, lane, k1, k2);
            if (lane == 0) { sm[32] = k1; sm[33] = k2; }
        }
        bar();
        STAMP(4);
        {
            const int ts = key_tok(sm[32 + (lane & 1)]);
            const u4 xr = *(const u4*)(x + ts * D + 64 * w + 32 * ((lane >> 1) & 1) + 8 * (lane >> 4));
            u64 sv;
            for (u32 spins = 0; spins < SPIN_CAP; ++spins) {
                sv = __hip_atomic_load((gu64*)(ws + WS_STATS) + ts, RLX_AGENT);
                if ((u32)(sv >> 32) == tag) break;
            }
            const float m = bfhi((u32)sv), rs = rcp(bflo((u32)sv));
            u4 a;
#pragma unroll
            for (int k = 0; k < 4; k++) {
                const u32 dd = pkbf(bflo(xr[k]) - m, bfhi(xr[k]) - m);
                a[k] = pkbf(bflo(dd) * rs, bfhi(dd) * rs);
            }
            STAMP(5);
            const f4 z = {0.f, 0.f, 0.f, 0.f};
            LAS float* red = (LAS float*)(lds + 98304);
#pragma unroll
            for (int c = 0; c < 2; c++) {
                const f4 g0 = mfma16b(a, bg[0][0][c], z), g1 = mfma16b(a, bg[0][1][c], z);
                const f4 u0 = mfma16b(a, bg[1][0][c], z), u1 = mfma16b(a, bg[1][1][c], z);
                if (lane < 16) {
                    *(LAS f2*)(red + w * 128 + (16 * c + lane) * 2) = (f2){g0[0] + g1[2], u0[0] + u1[2]};
                    *(LAS f2*)(red + w * 128 + 64 + (16 * c + lane) * 2) = (f2){g0[1] + g1[3], u0[1] + u1[3]};
                }
            }
            bar();
            f2 gu = {0.f, 0.f};
#pragma unroll
            for (int k = 0; k < 8; k++) gu += *(LAS f2*)(red + k * 128 + lane * 2);
            const float gb = rbf(gu.x);
            const float h = rbf(gb * sigm(gb)) * rbf(gu.y);
            LAS u16* hl = (LAS u16*)(lds + 98304 + 4096 + w * 128);
            hl[lane] = (u16)pkbf(h, h);
            const u4 ha = *(LAS u4*)(hl + (lane & 1) * 32 + (lane >> 4) * 8);
            const f4 d0 = mfma16b(ha, bd[0], z), d1 = mfma16b(ha, bd[1], z), d2 = mfma16b(ha, bd[2], z), d3 = mfma16b(ha, bd[3], z);
            const int g = lane >> 4;
            const float y0 = g == 0 ? d0[0] : g == 1 ? d1[0] : g == 2 ? d2[0] : d3[0];
            const float y1 = g == 0 ? d0[1] : g == 1 ? d1[1] : g == 2 ? d2[1] : d3[1];
            gu64* pp = (gu64*)(ws + WS_PARTS) + fp * 1024 + tid;
            __hip_atomic_store(pp, gran(tag, __float_as_uint(y0)), RLX_AGENT);
            __hip_atomic_store(pp + 512, gran(tag, __float_as_uint(y1)), RLX_AGENT);
        }
        STAMP(6);
        return;
    }
    const int d0 = idx * 4, c = w & 3;
    const u32 la = *(const u32*)(logA + (d0 + c) * NM + 2 * lane), ai = *(const u32*)(Aim + (d0 + c) * NM + 2 * lane);
    const u32 ldt = logdt[d0 + c];
    LAS char* kop = lds + O_KOP + c * KCH;
    LAS u32* misc = (LAS u32*)(lds + O_MISC);
    if (w >= 4) {
        const int e = tid - 256;
        u2 xv[8];
#pragma unroll
        for (int k = 0; k < 8; k++) xv[k] = *(const u2*)(x + (k * 256 + e) * D + d0);
        const u2 dsk = *(const u2*)(dskip + d0);
        asm volatile("s_load_dword %0, %1, 0\n\ts_waitcnt lgkmcnt(0)" : "=s"(tag) : "s"(ws) : "memory");
        tag += 1;
        *(LAS u2*)(lds + O_U + c * UCH + lane * 8) = (u2){0u, 0u};
        {
            const float dt = rbf(ex2(LOG2E * bflo(ldt)));
            const float xa = -rbf(ex2(LOG2E * bflo(la))) * dt * LOG2E, xb = -rbf(ex2(LOG2E * bfhi(la))) * dt * LOG2E;
            const float ta = bflo(ai) * dt * 0.15915494309189535f, tb = bfhi(ai) * dt * 0.15915494309189535f;
            const f2 wa = cexp(xa, ta, 8.f), wb = cexp(xb, tb, 8.f), war = rot(wa), wbr = rot(wb);
            f2 pa = wa, pb = wb;
            LAS char* o = kop + 21 * KROW + lane * 8;
            *(LAS u2*)o = (u2){0x00003c00u, 0x00003c00u};
#pragma unroll
            for (int b = 1; b < 32; b++) {
                *(LAS u2*)(o + b * KROW) = (u2){pkh(pa.x, pa.y), pkh(pb.x, pb.y)};
                if (b < 31) { pa = cmul(pa, wa, war); pb = cmul(pb, wb, wbr); }
            }
        }
        STAMP(1);
        bar();
        u64 sv[8];
        for (u32 spins = 0; spins < SPIN_CAP; ++spins) {
            bool ok = true;
#pragma unroll
            for (int k = 0; k < 8; k++) { sv[k] = __hip_atomic_load((gu64*)(ws + WS_STATS) + k * 256 + e, RLX_AGENT); ok &= (u32)(sv[k] >> 32) == tag; }
            if (ok) break;
            __builtin_amdgcn_s_sleep(1);
        }
        STAMP(2);
        const float dk[4] = {bflo(dsk.x), bfhi(dsk.x), bflo(dsk.y), bfhi(dsk.y)};
        float ea[2][4], eb[2][4];
        LAS u16* up = (LAS u16*)(lds + O_U) + 256 + e;
        LAS float* yp = (LAS float*)(lds + O_Y) + e + 4 * (e >> 4);
        f4 sc; int sel0, sel1;
#pragma unroll
        for (int k = 0; k <= 9; k++) {
            if (k >= 2) {
                const int t = (k - 2) * 256 + e;
                float o[4];
#pragma unroll
                for (int i = 0; i < 4; i++) o[i] = fmaf(yp[i * 2560 + (k - 2) * 320], eb[k & 1][i] * sc[i], ea[k & 1][i]);
                if (t != sel0 && t != sel1) *(u2*)(out + t * D + d0) = (u2){pkbf(o[0], o[1]), pkbf(o[2], o[3])};
            }
            if (k < 8) {
                const float m = bfhi((u32)sv[k]), rs = rcp(bflo((u32)sv[k]));
                const float xf[4] = {bflo(xv[k].x), bfhi(xv[k].x), bflo(xv[k].y), bfhi(xv[k].y)};
                float xn[4];
#pragma unroll
                for (int i = 0; i < 4; i++) xn[i] = (xf[i] - m) * rs;
                const u32 p0 = pkh(xn[0], xn[1]), p1 = pkh(xn[2], xn[3]);
                up[k * 256] = (u16)p0; up[2304 + k * 256] = (u16)(p0 >> 16); up[2 * 2304 + k * 256] = (u16)p1; up[3 * 2304 + k * 256] = (u16)(p1 >> 16);
                if (k == 0) { bar(); sc = *(LAS f4*)(misc + 4); sel0 = key_tok(misc[0]); sel1 = key_tok(misc[1]); STAMP(3); }
#pragma unroll
                for (int i = 0; i < 4; i++) { const float g = xn[i] * sigm(xn[i]); eb[k & 1][i] = g; ea[k & 1][i] = fmaf(xn[i] * dk[i], g, xf[i]); }
            }
            if (k >= 1 && k <= 8) bar();
            if (k == 2) STAMP(4);
        }
        STAMP(5);
        return;
    }
    const u32 cr = *(const u32*)(Cre + (d0 + c) * NM + 2 * lane), ci = *(const u32*)(Cim + (d0 + c) * NM + 2 * lane);
    asm volatile("s_load_dword %0, %1, 0\n\ts_waitcnt lgkmcnt(0)" : "=s"(tag) : "s"(ws) : "memory");
    tag += 1;
    if (tid == 0) __hip_atomic_store((gu64*)(ws + WS_ACKS) + idx * 16, gran(tag, 0u), RLX_AGENT);
    LAS char* ka = lds + O_K + c * KA;
    {
#pragma unroll
        for (int k = 0; k < 4; k++) if (k < 3 || lane < 48) *(LAS u4*)(ka + k * 1024 + lane * 16) = (u4){0u, 0u, 0u, 0u};
        const int sg = lane >> 4, o = lane & 15;
        const int lo = sg == 0 ? 11 : sg == 1 ? 6 : sg == 2 ? 5 : 4, hi = sg == 0 ? 13 : sg == 1 ? 10 : sg == 2 ? 11 : 12;
        if (o >= lo && o <= hi) {
            const int q = 559 + 32 * sg + o;
            *(LAS u16*)(ka + 2 * q) = 0x3c00; *(LAS u16*)(ka + 1472 + 2 * q + 2) = 0x3c00;
        }
    }
    {
        const float dt = rbf(ex2(LOG2E * bflo(ldt)));
        const float xa = -rbf(ex2(LOG2E * bflo(la))) * dt * LOG2E, xb = -rbf(ex2(LOG2E * bfhi(la))) * dt * LOG2E;
        const float ta = bflo(ai) * dt * 0.15915494309189535f, tb = bfhi(ai) * dt * 0.15915494309189535f;
        u32 mx = max(max(cr & 0x7fffu, (cr >> 16) & 0x7fffu), max(ci & 0x7fffu, (ci >> 16) & 0x7fffu));
        mx = wavemax(mx) >> 7;
        mx = min(max(mx, 20u), 240u);
        const float S = __uint_as_float((260u - mx) << 23);
        if (lane == 0) misc[4 + c] = __float_as_uint(2.f * dt * __uint_as_float((mx - 6u) << 23));
        const f2 za = cexp(xa, ta, 1.f), zb = cexp(xb, tb, 1.f), zar = rot(za), zbr = rot(zb);
        const f2 va = cexp(xa, ta, 256.f), vb = cexp(xb, tb, 256.f), var_ = rot(va), vbr = rot(vb);
        LAS char* o = kop + lane * 8;
        auto put = [&](int row, f2 a, f2 b) { *(LAS u2*)(o + row * KROW) = (u2){pkh(a.x, -a.y), pkh(b.x, -b.y)}; };
        f2 pa = {bflo(cr) * S, bflo(ci) * S}, pb = {bfhi(cr) * S, bfhi(ci) * S};
        put(0, pa, pb);
        f2 qa[2], qb[2];
#pragma unroll
        for (int i = 1; i <= 8; i++) {
            pa = cmul(pa, za, zar); pb = cmul(pb, zb, zbr);
            put(i < 8 ? i : 20, pa, pb);
            if ((i & 1) == 0) {
                const f2 ra = cmul(pa, va, var_), rb = cmul(pb, vb, vbr);
                put(7 + i / 2, ra, rb);
                if ((i & 3) == 0) { qa[i / 4 - 1] = ra; qb[i / 4 - 1] = rb; }
            }
        }
#pragma unroll
        for (int a = 0; a < 2; a++)
#pragma unroll
            for (int i = 0; i < 2; i++) { qa[i] = cmul(qa[i], va, var_); qb[i] = cmul(qb[i], vb, vbr); put(12 + 2 * a + i, qa[i], qb[i]); }
#pragma unroll
        for (int a = 0; a < 4; a++) { qa[1] = cmul(qa[1], va, var_); qb[1] = cmul(qb[1], vb, vbr); put(16 + a, qa[1], qb[1]); }
    }
    STAMP(1);
    bar();
    {
        f16v kd;
#pragma unroll
        for (int i = 0; i < 16; i++) kd[i] = 0.f;
        LAS char* pa = kop + (lane & 31) * KROW + (lane >> 5) * 16;
#pragma unroll
        for (int s = 0; s < 16; s++) {
            const u4 a = *(LAS u4*)(pa + s * 32), b = *(LAS u4*)(pa + 21 * KROW + s * 32);
            kd = __builtin_amdgcn_mfma_f32_32x32x16_f16(__builtin_bit_cast(h8, a), __builtin_bit_cast(h8, b), kd, 0, 0, 0);
        }
        const int be = lane & 31, h = lane >> 5;
        LAS char* r0 = ka; LAS char* r1 = ka + 1472 + 2;
        {
            const int q = 540 - 8 * be - 4 * h;
            const u32 a = pkh(kd[3], kd[2]), b = pkh(kd[1], kd[0]), m = pkh(kd[2], kd[1]);
            *(LAS u2*)(r0 + 2 * q) = (u2){a, b};
            *(LAS u16*)(r1 + 2 * q) = (u16)a; *(LAS u32*)(r1 + 2 * q + 2) = m; *(LAS u16*)(r1 + 2 * q + 6) = (u16)(b >> 16);
        }
        if (h == 0) {
            const int q = 278 - 8 * be;
            const u32 dd = pkh(kd[7], kd[7]), dc = pkh(kd[7], kd[6]), bb = pkh(kd[5], kd[5]), ba = pkh(kd[5], kd[4]), cb = pkh(kd[6], kd[5]);
            LAS u32* p0 = (LAS u32*)(r0 + 2 * q);
            p0[0] = dd; p0[1] = dc; p0[2] = bb; p0[3] = ba;
            *(LAS u16*)(r1 + 2 * q) = (u16)dd;
            LAS u32* p1 = (LAS u32*)(r1 + 2 * q + 2);
            p1[0] = dd; p1[1] = cb; p1[2] = bb;
            *(LAS u16*)(r1 + 2 * q + 14) = (u16)(ba >> 16);
            LAS u16* p3 = (LAS u16*)(ka + 2944 + 512 + 64) + be;
            const u32 k01 = pkh(kd[8], kd[9]), k23 = pkh(kd[10], kd[11]);
            p3[0] = (u16)k01; p3[32] = (u16)(k01 >> 16); p3[64] = (u16)k23; p3[96] = (u16)(k23 >> 16);
            if (be == 31) { *(LAS u16*)(r0 + 2 * 29) = (u16)dd; *(LAS u16*)(r1 + 2 * 29) = (u16)dd; }
        } else {
            LAS u32* p2 = (LAS u32*)(ka + 2944 + 128) + be;
            p2[0] = pkh(kd[4], kd[5]); p2[32] = pkh(kd[6], kd[7]);
            if (be == 31) {
                const u32 k256 = pkh(kd[8], kd[8]), k1024 = pkh(kd[7], kd[7]);
                *(LAS u32*)(r0 + 2 * 286) = k256; *(LAS u16*)(r1 + 2 * 286) = (u16)k256; *(LAS u16*)(r1 + 2 * 287) = (u16)k256;
                *(LAS u16*)(r0 + 2 * 699) = (u16)k1024; *(LAS u16*)(r0 + 2 * 700) = (u16)k1024; *(LAS u32*)(r1 + 2 * 699) = k1024;
            }
        }
    }
    u4 KF[22], K2[3][2], K3[4];
    {
        const int j = lane & 15, g = lane >> 4;
        LAS u32* kb = (LAS u32*)(ka + ((j & 1) ? 0 : 1472 + 2) + 2 * (15 - j + 8 * g));
#pragma unroll
        for (int m = 0; m < 22; m++) { LAS u32* p = kb + 16 * (21 - m); KF[m] = (u4){p[0], p[1], p[2], p[3]}; }
        LAS u32* p2 = (LAS u32*)(ka + 2944 + 128) + 2 * (j - 4 * g - 3);
#pragma unroll
        for (int s = 0; s < 3; s++)
#pragma unroll
            for (int pi = 0; pi < 2; pi++) { LAS u32* p = p2 + 32 * s + pi; K2[s][pi] = (u4){p[6], p[4], p[2], p[0]}; }
        LAS u32* p3 = (LAS u32*)(ka + 2944 + 512 + 64) + (j - 4 * g - 3);
#pragma unroll
        for (int s = 0; s < 4; s++) { LAS u32* p = p3 + 16 * s; K3[s] = (u4){p[3], p[2], p[1], p[0]}; }
    }
    STAMP(2);
    u32 k1 = 0, k2 = 0; u32 xsel = 0;
    if (w == 0) {
        gather_top2(ws, tag, lane, k1, k2);
        if (lane == 0) { misc[0] = k1; misc[1] = k2; }
        xsel = x[key_tok((lane & 4) ? k2 : k1) * D + d0 + (lane & 3)];
    }
    STAMP(3);
    {
        LAS char* ub = lds + O_U + c * UCH + 2 * (256 - 16 + 16 * (lane & 15) + 8 * (lane >> 4));
        LAS char* yb = lds + O_Y + c * YCH + (lane & 15) * 80 + (lane >> 4) * 16;
        const f4 z = {0.f, 0.f, 0.f, 0.f};
        f4 acc[8];
#pragma unroll
        for (int i = 0; i < 8; i++) acc[i] = z;
        u4 Fr[8], G2[6][2], G3[4];
#pragma unroll
        for (int k = 0; k < 8; k++) {
            bar();
            if (k == 0) STAMP(4);
#pragma unroll
            for (int b = 0; b < 6; b++) if (k - b >= 2 && k - b <= 4) { acc[k] = mfma16(G2[b][0], K2[k - b - 2][0], acc[k]); acc[k] = mfma16(G2[b][1], K2[k - b - 2][1], acc[k]); }
#pragma unroll
            for (int b = 0; b < 4; b++) if (k - b >= 4) acc[k] = mfma16(G3[b], K3[k - b - 4], acc[k]);
            if (k >= 1) {
                const int b = k - 1;
                if (b <= 5) {
                    const u4 Fs = *(LAS u4*)(ub + 2 * (256 * b - 8));
                    const f4 v1 = mfma16(Fr[0], KF[4], z), v2 = mfma16(Fr[0], KF[3], z), v3 = mfma16(Fs, KF[4], z), v4 = mfma16(Fs, KF[3], z);
                    G2[b][0] = (u4){pkh(v1[0], v2[0]), pkh(v1[1], v2[1]), pkh(v1[2], v2[2]), pkh(v1[3], v2[3])};
                    G2[b][1] = (u4){pkh(v3[0], v4[0]), pkh(v3[1], v4[1]), pkh(v3[2], v4[2]), pkh(v3[3], v4[3])};
                    if (b <= 3) {
                        const f4 v5 = mfma16(Fr[0], KF[2], z), v6 = mfma16(Fs, KF[1], z);
                        G3[b] = (u4){pkh(v5[0], v6[0]), pkh(v5[1], v6[1]), pkh(v5[2], v6[2]), pkh(v5[3], v6[3])};
                        acc[b + 4] = mfma16(KF[0], Fr[0], acc[b + 4]);
                    }
                    acc[b + 2] = mfma16(KF[21], Fr[0], acc[b + 2]);
                }
#pragma unroll
                for (int m = 0; m < 8; m++) acc[k] = mfma16(KF[13 + m], Fr[m], acc[k]);
            }
#pragma unroll
            for (int m = 0; m < 8; m++) Fr[m] = *(LAS u4*)(ub + 2 * (256 * k - 32 * m));
#pragma unroll
            for (int m = 0; m < 8; m++) acc[k] = mfma16(KF[5 + m], Fr[m], acc[k]);
            *(LAS f4*)(yb + k * 1280) = acc[k];
        }
    }
    STAMP(5);
    bar();
    if (w == 0) {
        const int tk = (lane >> 2) & 1, ch = lane & 3, pg = lane >> 3;
        gu64* pp = (gu64*)(ws + WS_PARTS) + pg * 1024 + tk * 512 + d0 + ch;
        u64 v[6];
        const bool last = pg < 3;
        for (u32 spins = 0; spins < SPIN_CAP; ++spins) {
            bool ok = true;
#pragma unroll
            for (int k = 0; k < 6; k++) { v[k] = __hip_atomic_load(pp + ((k < 5 || last) ? k : 0) * 8192, RLX_AGENT); ok &= (u32)(v[k] >> 32) == tag; }
            if (ok) break;
            __builtin_amdgcn_s_sleep(1);
        }
        float y = last ? __uint_as_float((u32)v[5]) : 0.f;
#pragma unroll
        for (int k = 0; k < 5; k++) y += __uint_as_float((u32)v[k]);
        y += dppf<0x128>(y);
        y += __shfl_xor(y, 16); y += __shfl_xor(y, 32);
        const u32 key = tk ? k2 : k1;
        const int t = key_tok(key);
        const float yf = rbf(rbf(y) * rbf(sigm(key_r(key))));
        const float xn = rbf((float)*(LAS _Float16*)(lds + O_U + ch * UCH + 2 * (256 + t)));
        const float bb = rbf(xn + yf);
        const float g = rbf(bb * sigm(bb));
        const float yc = *(LAS float*)(lds + O_Y + ch * YCH + 4 * (t + 4 * (t >> 4))) * __uint_as_float(misc[4 + ch]);
        const float s = rbf(rbf(yc) + rbf(xn * bflo(dskip[d0 + ch])));
        if (lane < 8) out[t * D + d0 + ch] = (u16)pkbf(bflo(xsel) + rbf(s * g), 0.f);
        STAMP(6);
    }
}

extern "C" void kernel_launch(void* const* d_in, const int* in_sizes, int n_in,
                              void* d_out, int out_size, void* d_ws, size_t ws_size,
                              hipStream_t stream) {
    s4d_mod_kernel<<<256, 512, 0, stream>>>((const u16*)d_in[0], (const u16*)d_in[1], (const u16*)d_in[2], (const u16*)d_in[3], (const u16*)d_in[4],
        (const u16*)d_in[5], (const u16*)d_in[6], (const u16*)d_in[8], (const u16*)d_in[9], (const u16*)d_in[10], (const u16*)d_in[11],
        (u16*)d_out, (char*)d_ws);
}
```

```cpp
#include <hip/hip_runtime.h>

__global__ void __launch_bounds__(512, 2) s4d_mod_kernel(const void* x, const void* logA, const void* Aim, const void* Cre, const void* Cim,
        const void* logdt, const void* dskip, const void* wr, const void* wg, const void* wu, const void* wd, void* out, void* ws) {
    __shared__ __attribute__((aligned(16))) char smem[160960];
    asm volatile(
        ".Ls4d_KSTART:\n"
        "s_load_dwordx16 s[4:19], s[0:1], 0\n"
        "s_load_dwordx8 s[20:27], s[0:1], 64\n"
        "s_load_dwordx2 s[28:29], s[0:1], 96\n"
        "s_getpc_b64 s[42:43]\n"
        ".Ls4d_KPC:\n"
        "v_readfirstlane_b32 s30, v0\n"
        "s_and_b32 s31, s2, 7\n"
        "s_lshr_b32 s35, s2, 3\n"
        "s_lshr_b32 s45, s2, 4\n"
        "s_lshl_b32 s34, s31, 4\n"
        "s_add_u32 s34, s34, s45\n"
        "s_lshr_b32 s30, s30, 6\n"
        "s_lshl_b32 s3, s35, 3\n"
        "s_add_u32 s3, s3, s30\n"
        "s_lshl_b32 s3, s3, 7\n"
        "s_min_u32 s3, s3, .Ls4d_KEND-.Ls4d_KPC-4\n"
        "v_mov_b32 v255, s3\n"
        "global_load_dword v255, v255, s[42:43]\n"
        "s_bitcmp1_b32 s2, 3\n"
        "s_cbranch_scc1 .Ls4d_L_AUX0\n"
        "v_and_b32 v6, 0xff, v0\n"
        "s_lshl_b32 s3, s34, 10\n"
        "s_lshl_b32 s36, s34, 3\n"
        "v_lshl_add_u32 v1, v6, 2, s3\n"
        "v_lshlrev_b32 v72, 11, v6\n"
        "s_cmp_ge_u32 s30, 4\n"
        "s_waitcnt lgkmcnt(0)\n"
        "global_load_dword v2, v1, s[6:7]\n"
        "global_load_dword v3, v1, s[8:9]\n"
        "s_load_dwordx2 s[38:39], s[14:15], s36\n"
        "s_load_dwordx2 s[40:41], s[16:17], s36\n"
        "s_load_dword s33, s[28:29], 0\n"
        "s_cbranch_scc1 .Ls4d_L_E0\n"
        "global_load_dword v4, v1, s[10:11]\n"
        "global_load_dword v5, v1, s[12:13]\n"
        "s_lshl_b32 s3, s45, 17\n"
        "s_lshl_b32 s48, s31, 7\n"
        "s_add_u32 s3, s3, s48\n"
        "v_lshrrev_b32 v254, 1, v6\n"
        "v_lshl_add_u32 v254, v254, 10, s3\n"
        "s_barrier\n"
        "global_load_dword v254, v254, s[4:5]\n"
        "s_branch .Ls4d_L_M\n"
        ".Ls4d_L_E0:\n"
        "s_add_u32 s4, s4, s36\n"
        "s_addc_u32 s5, s5, 0\n"
        "v_add_u32 v73, 0x80000, v72\n"
        "s_barrier\n"
        "s_lshl_b32 s3, s31, 14\n"
        "s_lshl_b32 s48, s45, 10\n"
        "s_add_u32 s3, s3, s48\n"
        "s_add_u32 s3, s3, 4096\n"
        "v_lshrrev_b32 v254, 5, v6\n"
        "v_lshl_add_u32 v254, v254, 7, s3\n"
        "v_and_b32 v253, 1, v0\n"
        "v_lshl_add_u32 v254, v253, 17, v254\n"
        "global_load_dword v254, v254, s[28:29] sc1\n"
        "s_branch .Ls4d_L_E\n"
        ".Ls4d_L_AUX0:\n"
        "v_and_b32 v3, 63, v0\n"
        "s_lshl_b32 s3, s34, 14\n"
        "s_lshl_b32 s39, s30, 11\n"
        "s_add_u32 s3, s3, s39\n"
        "v_lshlrev_b32 v2, 4, v3\n"
        "v_add_u32 v1, s3, v2\n"
        "s_waitcnt lgkmcnt(0)\n"
        "global_load_dwordx4 v[4:7], v2, s[18:19]\n"
        "global_load_dwordx4 v[8:11], v1, s[4:5] offset:0\n"
        "global_load_dwordx4 v[12:15], v1, s[4:5] offset:1024\n"
        "s_load_dword s33, s[28:29], 0\n"
        "v_mov_b32 v28, 0\n"
        "v_mov_b32 v29, 0\n"
        "v_mov_b32 v30, 0\n"
        "v_mov_b32 v31, 0\n"
        "v_mov_b32 v32, 0\n"
        "v_mov_b32 v33, 0\n"
        "v_mov_b32 v34, 0\n"
        "v_lshrrev_b32 v44, 4, v0\n"
        "v_lshlrev_b32 v44, 3, v44\n"
        "v_add_u32 v44, 0x272c0, v44\n"
        "v_and_b32 v45, 15, v0\n"
        "v_lshlrev_b32 v46, 4, v45\n"
        "v_add_u32 v46, 0x272c0, v46\n"
        "s_lshl_b32 s39, s34, 7\n"
        "s_add_u32 s3, s39, 4096\n"
        "v_lshl_add_u32 v47, v45, 3, s3\n"
        "v_lshrrev_b32 v49, 4, v3\n"
        "v_lshl_add_u32 v47, v49, 14, v47\n"
        "v_add_u32 v50, 0x10000, v47\n"
        "s_add_u32 s3, s39, 135168\n"
        "v_and_b32 v48, 1, v0\n"
        "v_lshl_add_u32 v48, v48, 3, s3\n"
        "v_lshrrev_b32 v49, 1, v45\n"
        "v_lshl_add_u32 v48, v49, 14, v48\n"
        "s_lshl_b32 s3, s34, 4\n"
        "s_sub_u32 s3, 2047, s3\n"
        "v_sub_u32 v45, s3, v45\n"
        "s_mul_i32 s38, s31, 6\n"
        "s_add_u32 s38, s38, s45\n"
        "s_cmp_lt_u32 s45, 6\n"
        "s_cselect_b32 s3, s38, 99\n"
        "s_cmp_lt_u32 s3, 43\n"
        "s_cselect_b32 s37, 1, 0\n"
        "s_cmp_eq_u32 s34, 127\n"
        "s_cselect_b32 s37, 2, s37\n"
        "s_mul_i32 s48, s45, 3\n"
        "s_mul_i32 s49, s45, 7\n"
        "s_add_u32 s49, s49, 4294967272\n"
        "s_cmp_lt_u32 s45, 6\n"
        "s_cselect_b32 s48, s48, s49\n"
        "s_cselect_b32 s49, 3, 7\n"
        "s_add_u32 s50, s30, 7\n"
        "s_and_b32 s50, s50, 7\n"
        "s_add_u32 s48, s48, s50\n"
        "s_min_u32 s48, s48, 87\n"
        "s_cmp_lt_u32 s48, 64\n"
        "s_cbranch_scc1 .Ls4d_L_PFA1\n"
        "s_mul_i32 s3, s31, 24\n"
        "s_add_u32 s3, s3, s48\n"
        "s_sub_u32 s3, s3, 64\n"
        "s_lshl_b32 s3, s3, 13\n"
        "s_mov_b64 s[60:61], s[24:25]\n"
        "v_lshl_add_u32 v24, v3, 7, s3\n"
        "s_branch .Ls4d_L_PFB1\n"
        ".Ls4d_L_PFA1:\n"
        "s_and_b32 s3, s48, 31\n"
        "s_mul_i32 s3, s3, 44032\n"
        "s_mul_i32 s62, s31, 384\n"
        "s_add_u32 s3, s3, s62\n"
        "s_bitcmp1_b32 s48, 5\n"
        "s_cselect_b64 s[60:61], s[22:23], s[20:21]\n"
        "v_lshrrev_b32 v24, 2, v3\n"
        "v_and_b32 v25, 3, v0\n"
        "v_mul_u32_u24 v24, 0xac0, v24\n"
        "v_lshlrev_b32 v25, 7, v25\n"
        "v_min_u32 v25, 380, v25\n"
        "v_add3_u32 v24, v24, v25, s3\n"
        ".Ls4d_L_PFB1:\n"
        "v_min_u32 v24, 0x157ffc, v24\n"
        "s_min_u32 s51, s49, 3\n"
        "s_waitcnt vmcnt(2)\n"
        "v_dot2c_f32_bf16 v34, 0x3f803f80, v4\n"
        "v_dot2c_f32_bf16 v34, 0x3f803f80, v5\n"
        "v_dot2c_f32_bf16 v34, 0x3f803f80, v6\n"
        "v_dot2c_f32_bf16 v34, 0x3f803f80, v7\n"
        "s_nop 2\n"
        "v_mov_b32 v35, v34\n"
        "s_nop 1\n"
        "v_permlane32_swap_b32 v34, v35\n"
        "v_add_f32 v34, v34, v35\n"
        "s_waitcnt vmcnt(1)\n"
        "v_dot2c_f32_bf16 v28, 0x3f803f80, v8\n"
        "v_dot2c_f32_bf16 v30, v8, v8\n"
        "v_dot2c_f32_bf16 v32, v8, v4\n"
        "v_dot2c_f32_bf16 v28, 0x3f803f80, v9\n"
        "v_dot2c_f32_bf16 v30, v9, v9\n"
        "v_dot2c_f32_bf16 v32, v9, v5\n"
        "v_dot2c_f32_bf16 v28, 0x3f803f80, v10\n"
        "v_dot2c_f32_bf16 v30, v10, v10\n"
        "v_dot2c_f32_bf16 v32, v10, v6\n"
        "v_dot2c_f32_bf16 v28, 0x3f803f80, v11\n"
        "v_dot2c_f32_bf16 v30, v11, v11\n"
        "v_dot2c_f32_bf16 v32, v11, v7\n"
        "s_waitcnt vmcnt(0)\n"
        "s_cmp_lt_u32 s50, s51\n"
        "s_cbranch_scc0 .Ls4d_L_PFE\n"
        "global_load_dword v250, v24, s[60:61]\n"
        ".Ls4d_L_PFE:\n"
        "v_dot2c_f32_bf16 v29, 0x3f803f80, v12\n"
        "v_dot2c_f32_bf16 v31, v12, v12\n"
        "v_dot2c_f32_bf16 v33, v12, v4\n"
        "v_dot2c_f32_bf16 v29, 0x3f803f80, v13\n"
        "v_dot2c_f32_bf16 v31, v13, v13\n"
        "v_dot2c_f32_bf16 v33, v13, v5\n"
        "v_dot2c_f32_bf16 v29, 0x3f803f80, v14\n"
        "v_dot2c_f32_bf16 v31, v14, v14\n"
        "v_dot2c_f32_bf16 v33, v14, v6\n"
        "v_dot2c_f32_bf16 v29, 0x3f803f80, v15\n"
        "v_dot2c_f32_bf16 v31, v15, v15\n"
        "v_dot2c_f32_bf16 v33, v15, v7\n"
        "s_nop 0\n"
        "v_permlane32_swap_b32 v28, v29\n"
        "v_permlane32_swap_b32 v30, v31\n"
        "v_permlane32_swap_b32 v32, v33\n"
        "v_add_f32 v28, v28, v29\n"
        "v_add_f32 v30, v30, v31\n"
        "v_add_f32 v32, v32, v33\n"
        "s_nop 0\n"
        "v_permlane16_swap_b32 v28, v30\n"
        "v_permlane16_swap_b32 v32, v34\n"
        "v_add_f32 v40, v28, v30\n"
        "v_add_f32 v41, v32, v34\n"
        "s_nop 0\n"
        "v_add_f32_dpp v40, v40, v40 row_ror:8 row_mask:0xf bank_mask:0xf bound_ctrl:1\n"
        "v_add_f32_dpp v41, v41, v41 row_ror:8 row_mask:0xf bank_mask:0xf bound_ctrl:1\n"
        "s_nop 0\n"
        "v_add_f32_dpp v40, v40, v40 row_ror:4 row_mask:0xf bank_mask:0xf bound_ctrl:1\n"
        "v_add_f32_dpp v41, v41, v41 row_ror:4 row_mask:0xf bank_mask:0xf bound_ctrl:1\n"
        "s_nop 0\n"
        "v_add_f32_dpp v40, v40, v40 row_ror:2 row_mask:0xf bank_mask:0xf bound_ctrl:1\n"
        "v_add_f32_dpp v41, v41, v41 row_ror:2 row_mask:0xf bank_mask:0xf bound_ctrl:1\n"
        "s_nop 0\n"
        "v_add_f32_dpp v40, v40, v40 row_ror:1 row_mask:0xf bank_mask:0xf bound_ctrl:1\n"
        "v_add_f32_dpp v41, v41, v41 row_ror:1 row_mask:0xf bank_mask:0xf bound_ctrl:1\n"
        "s_mov_b32 exec_lo, 0x00010001\n"
        "s_mov_b32 exec_hi, 0x00010001\n"
        "ds_write_b64 v44, v[40:41]\n"
        "s_mov_b64 exec, -1\n"
        "s_cmp_eq_u32 s30, 0\n"
        "s_waitcnt lgkmcnt(0)\n"
        "s_barrier\n"
        "s_cbranch_scc0 .Ls4d_L_ROW1\n"
        "ds_read_b128 v[4:7], v46\n"
        "s_add_u32 s33, s33, 1\n"
        "v_mov_b32 v9, s33\n"
        "v_mov_b32 v11, s33\n"
        "s_mov_b32 s3, 0xffff0000\n"
        "s_waitcnt lgkmcnt(0)\n"
        "v_mul_f32 v4, 0x3b000000, v4\n"
        "v_mul_f32 v6, 0x3b000000, v6\n"
        "v_fma_f32 v6, -v4, v4, v6\n"
        "v_max_f32 v6, 0, v6\n"
        "v_cvt_pk_bf16_f32 v6, v6, v6\n"
        "v_and_b32 v6, s3, v6\n"
        "v_add_f32 v6, 0x3727c5ac, v6\n"
        "v_cvt_pk_bf16_f32 v6, v6, v6\n"
        "v_and_b32 v6, s3, v6\n"
        "v_sqrt_f32 v6, v6\n"
        "s_nop 0\n"
        "v_cvt_pk_bf16_f32 v8, v6, v4\n"
        "global_store_dwordx2 v47, v[8:9], s[28:29] sc1\n"
        "global_store_dwordx2 v50, v[8:9], s[28:29] sc1\n"
        "v_lshlrev_b32 v6, 16, v8\n"
        "v_rcp_f32 v6, v6\n"
        "v_and_b32 v4, s3, v8\n"
        "v_fma_f32 v5, -v4, v7, v5\n"
        "v_mul_f32 v5, v5, v6\n"
        "v_cvt_pk_bf16_f32 v5, v5, v5\n"
        "v_ashrrev_i32 v6, 31, v5\n"
        "v_or_b32 v6, 0x80000000, v6\n"
        "v_xor_b32 v5, v5, v6\n"
        "v_and_or_b32 v5, v5, s3, v45\n"
        "s_nop 1\n"
        "v_max_u32_dpp v6, v5, v5 row_ror:8 row_mask:0xf bank_mask:0xf bound_ctrl:1\n"
        "s_nop 1\n"
        "v_max_u32_dpp v6, v6, v6 row_ror:4 row_mask:0xf bank_mask:0xf bound_ctrl:1\n"
        "s_nop 1\n"
        "v_max_u32_dpp v6, v6, v6 row_ror:2 row_mask:0xf bank_mask:0xf bound_ctrl:1\n"
        "s_nop 1\n"
        "v_max_u32_dpp v6, v6, v6 row_ror:1 row_mask:0xf bank_mask:0xf bound_ctrl:1\n"
        "v_cmp_ne_u32 vcc, v5, v6\n"
        "s_nop 1\n"
        "v_cndmask_b32 v5, 0, v5, vcc\n"
        "s_nop 1\n"
        "v_max_u32_dpp v5, v5, v5 row_ror:8 row_mask:0xf bank_mask:0xf bound_ctrl:1\n"
        "s_nop 1\n"
        "v_max_u32_dpp v5, v5, v5 row_ror:4 row_mask:0xf bank_mask:0xf bound_ctrl:1\n"
        "s_nop 1\n"
        "v_max_u32_dpp v5, v5, v5 row_ror:2 row_mask:0xf bank_mask:0xf bound_ctrl:1\n"
        "s_nop 1\n"
        "v_max_u32_dpp v5, v5, v5 row_ror:1 row_mask:0xf bank_mask:0xf bound_ctrl:1\n"
        "s_mov_b64 vcc, 0x5555\n"
        "v_cndmask_b32 v10, v5, v6, vcc\n"
        "s_mov_b64 exec, 0xffff\n"
        "global_store_dwordx2 v48, v[10:11], s[28:29] sc1\n"
        "s_mov_b64 exec, -1\n"
        "s_branch .Ls4d_L_ROW2\n"
        ".Ls4d_L_ROW1:\n"
        "s_cmp_ge_u32 s30, 4\n"
        "s_cselect_b32 s3, s37, 0\n"
        "s_cmp_eq_u32 s3, 1\n"
        "s_cbranch_scc1 .Ls4d_L_FFN\n"
        ".Ls4d_L_ROW2:\n"
        "s_barrier\n"
        "s_cmp_lt_u32 s50, s51\n"
        "s_cbranch_scc1 .Ls4d_L_PFL\n"
        "s_cmp_lt_u32 s50, s49\n"
        "s_cbranch_scc0 .Ls4d_L_PFL\n"
        "global_load_dword v251, v24, s[60:61]\n"
        ".Ls4d_L_PFL:\n"
        "s_cmp_eq_u32 s37, 0\n"
        "s_cbranch_scc1 .Ls4d_L_AEND\n"
        "s_cmp_ge_u32 s30, 4\n"
        "s_cbranch_scc1 .Ls4d_L_WRK\n"
        "s_cmp_eq_u32 s30, 0\n"
        "s_cbranch_scc1 .Ls4d_L_SELW\n"
        ".Ls4d_L_AEND:\n"
        "s_endpgm\n"
        ".Ls4d_L_SELW:\n"
        "v_lshrrev_b32 v20, 1, v3\n"
        "v_and_b32 v21, 1, v0\n"
        "v_lshlrev_b32 v21, 3, v21\n"
        "v_lshl_add_u32 v20, v20, 7, v21\n"
        "s_lshl_b32 s39, s31, 14\n"
        "s_add_u32 s3, s39, 139264\n"
        "v_add_u32 v20, s3, v20\n"
        "v_add_u32 v21, 0x2000, v20\n"
        "s_add_u32 s39, s39, 4096\n"
        "v_add_u32 v22, 0x18000, v2\n"
        "v_mov_b32 v48, 0x272c0\n"
        "s_mov_b32 s3, 1048576\n"
        "s_sleep 6\n"
        ".Ls4d_L_KPOLL:\n"
        "global_load_dwordx2 v[32:33], v20, s[28:29] offset:-4096 sc1\n"
        "global_load_dwordx2 v[34:35], v20, s[28:29] offset:0 sc1\n"
        "global_load_dwordx2 v[36:37], v21, s[28:29] offset:-4096 sc1\n"
        "global_load_dwordx2 v[38:39], v21, s[28:29] offset:0 sc1\n"
        "s_sub_u32 s3, s3, 1\n"
        "s_cbranch_scc1 .Ls4d_L_KDONE\n"
        "s_waitcnt vmcnt(0)\n"
        "v_cmp_ne_u32 vcc, s33, v33\n"
        "v_cmp_ne_u32 s[50:51], s33, v35\n"
        "v_cmp_ne_u32 s[52:53], s33, v37\n"
        "v_cmp_ne_u32 s[54:55], s33, v39\n"
        "s_or_b64 vcc, vcc, s[50:51]\n"
        "s_or_b64 s[52:53], s[52:53], s[54:55]\n"
        "s_or_b64 exec, vcc, s[52:53]\n"
        "s_cbranch_execnz .Ls4d_L_KPOLL\n"
        ".Ls4d_L_KDONE:\n"
        "s_mov_b64 exec, -1\n"
        "s_waitcnt vmcnt(0)\n"
        "v_max_u32 v40, v32, v34\n"
        "v_max_u32 v41, v36, v38\n"
        "v_max_u32 v42, v40, v41\n"
        "v_min_u32 v40, v40, v41\n"
        "v_min_u32 v41, v32, v34\n"
        "v_max_u32_dpp v43, v42, v42 row_ror:8 row_mask:0xf bank_mask:0xf bound_ctrl:1\n"
        "v_min_u32 v44, v36, v38\n"
        "v_max3_u32 v40, v40, v41, v44\n"
        "v_max_u32_dpp v43, v43, v43 row_ror:4 row_mask:0xf bank_mask:0xf bound_ctrl:1\n"
        "v_mov_b32 v45, s33\n"
        "v_mov_b32 v47, s33\n"
        "v_max_u32_dpp v43, v43, v43 row_ror:2 row_mask:0xf bank_mask:0xf bound_ctrl:1\n"
        "s_nop 1\n"
        "v_max_u32_dpp v43, v43, v43 row_ror:1 row_mask:0xf bank_mask:0xf bound_ctrl:1\n"
        "s_nop 1\n"
        "v_max_u32_dpp v43, v43, v43 row_bcast:15 row_mask:0xa bank_mask:0xf\n"
        "s_nop 1\n"
        "v_max_u32_dpp v43, v43, v43 row_bcast:31 row_mask:0xc bank_mask:0xf\n"
        "s_nop 0\n"
        "v_readlane_b32 s50, v43, 63\n"
        "s_nop 0\n"
        "s_andn2_b32 s52, 0x7ff, s50\n"
        "v_cmp_eq_u32 vcc, s50, v42\n"
        "v_lshl_add_u32 v23, s52, 10, v2\n"
        "s_lshl_b32 s52, s52, 3\n"
        "s_add_u32 s52, s52, s39\n"
        "v_cndmask_b32 v42, v42, v40, vcc\n"
        "global_load_dwordx4 v[8:11], v23, s[4:5]\n"
        "v_mov_b32 v24, s52\n"
        "v_max_u32_dpp v46, v42, v42 row_ror:8 row_mask:0xf bank_mask:0xf bound_ctrl:1\n"
        "s_nop 1\n"
        "v_max_u32_dpp v46, v46, v46 row_ror:4 row_mask:0xf bank_mask:0xf bound_ctrl:1\n"
        "s_nop 1\n"
        "v_max_u32_dpp v46, v46, v46 row_ror:2 row_mask:0xf bank_mask:0xf bound_ctrl:1\n"
        "s_nop 1\n"
        "v_max_u32_dpp v46, v46, v46 row_ror:1 row_mask:0xf bank_mask:0xf bound_ctrl:1\n"
        "global_load_dwordx2 v[16:17], v24, s[28:29] sc1\n"
        "s_nop 0\n"
        "v_max_u32_dpp v46, v46, v46 row_bcast:15 row_mask:0xa bank_mask:0xf\n"
        "s_nop 1\n"
        "v_max_u32_dpp v46, v46, v46 row_bcast:31 row_mask:0xc bank_mask:0xf\n"
        "v_mov_b32 v44, s50\n"
        "v_readlane_b32 s51, v46, 63\n"
        "s_nop 0\n"
        "s_andn2_b32 s53, 0x7ff, s51\n"
        "v_lshl_add_u32 v23, s53, 10, v2\n"
        "s_lshl_b32 s53, s53, 3\n"
        "s_add_u32 s53, s53, s39\n"
        "global_load_dwordx4 v[12:15], v23, s[4:5]\n"
        "v_mov_b32 v25, s53\n"
        "global_load_dwordx2 v[18:19], v25, s[28:29] sc1\n"
        "s_mov_b32 exec_lo, 0\n"
        "s_brev_b32 exec_hi, 1\n"
        "ds_write_b128 v48, v[44:47]\n"
        "s_mov_b64 exec, -1\n"
        ".Ls4d_L_SPOLL:\n"
        "s_waitcnt vmcnt(0)\n"
        "v_cmp_ne_u32 vcc, s33, v17\n"
        "v_cmp_ne_u32 s[54:55], s33, v19\n"
        "s_nop 0\n"
        "s_or_b64 vcc, vcc, s[54:55]\n"
        "s_cbranch_vccz .Ls4d_L_SDONE\n"
        "s_sub_u32 s3, s3, 1\n"
        "s_cbranch_scc1 .Ls4d_L_SDONE\n"
        "global_load_dwordx2 v[16:17], v24, s[28:29] sc1\n"
        "global_load_dwordx2 v[18:19], v25, s[28:29] sc1\n"
        "s_branch .Ls4d_L_SPOLL\n"
        ".Ls4d_L_SDONE:\n"
        "s_mov_b32 s3, 0xffff0000\n"
        "v_lshlrev_b32 v17, 16, v16\n"
        "v_lshlrev_b32 v19, 16, v18\n"
        "v_and_b32 v16, s3, v16\n"
        "v_and_b32 v18, s3, v18\n"
        "v_rcp_f32 v17, v17\n"
        "v_rcp_f32 v19, v19\n"
        "v_lshlrev_b32 v32, 16, v8\n"
        "v_and_b32 v33, s3, v8\n"
        "v_lshlrev_b32 v34, 16, v9\n"
        "v_and_b32 v35, s3, v9\n"
        "v_lshlrev_b32 v36, 16, v10\n"
        "v_and_b32 v37, s3, v10\n"
        "v_lshlrev_b32 v38, 16, v11\n"
        "v_and_b32 v39, s3, v11\n"
        "v_pk_add_f32 v[32:33], v[32:33], v[16:17] op_sel_hi:[1,0] neg_lo:[0,1] neg_hi:[0,1]\n"
        "v_pk_add_f32 v[34:35], v[34:35], v[16:17] op_sel_hi:[1,0] neg_lo:[0,1] neg_hi:[0,1]\n"
        "v_pk_add_f32 v[36:37], v[36:37], v[16:17] op_sel_hi:[1,0] neg_lo:[0,1] neg_hi:[0,1]\n"
        "v_pk_add_f32 v[38:39], v[38:39], v[16:17] op_sel_hi:[1,0] neg_lo:[0,1] neg_hi:[0,1]\n"
        "v_cvt_pk_bf16_f32 v8, v32, v33\n"
        "v_cvt_pk_bf16_f32 v9, v34, v35\n"
        "v_cvt_pk_bf16_f32 v10, v36, v37\n"
        "v_cvt_pk_bf16_f32 v11, v38, v39\n"
        "v_lshlrev_b32 v32, 16, v8\n"
        "v_and_b32 v33, s3, v8\n"
        "v_lshlrev_b32 v34, 16, v9\n"
        "v_and_b32 v35, s3, v9\n"
        "v_lshlrev_b32 v36, 16, v10\n"
        "v_and_b32 v37, s3, v10\n"
        "v_lshlrev_b32 v38, 16, v11\n"
        "v_and_b32 v39, s3, v11\n"
        "v_pk_mul_f32 v[32:33], v[32:33], v[16:17] op_sel:[0,1] op_sel_hi:[1,1]\n"
        "v_pk_mul_f32 v[34:35], v[34:35], v[16:17] op_sel:[0,1] op_sel_hi:[1,1]\n"
        "v_pk_mul_f32 v[36:37], v[36:37], v[16:17] op_sel:[0,1] op_sel_hi:[1,1]\n"
        "v_pk_mul_f32 v[38:39], v[38:39], v[16:17] op_sel:[0,1] op_sel_hi:[1,1]\n"
        "v_cvt_pk_bf16_f32 v8, v32, v33\n"
        "v_cvt_pk_bf16_f32 v9, v34, v35\n"
        "v_cvt_pk_bf16_f32 v10, v36, v37\n"
        "v_cvt_pk_bf16_f32 v11, v38, v39\n"
        "ds_write_b128 v22, v[8:11] offset:0\n"
        "v_lshlrev_b32 v32, 16, v12\n"
        "v_and_b32 v33, s3, v12\n"
        "v_lshlrev_b32 v34, 16, v13\n"
        "v_and_b32 v35, s3, v13\n"
        "v_lshlrev_b32 v36, 16, v14\n"
        "v_and_b32 v37, s3, v14\n"
        "v_lshlrev_b32 v38, 16, v15\n"
        "v_and_b32 v39, s3, v15\n"
        "v_pk_add_f32 v[32:33], v[32:33], v[18:19] op_sel_hi:[1,0] neg_lo:[0,1] neg_hi:[0,1]\n"
        "v_pk_add_f32 v[34:35], v[34:35], v[18:19] op_sel_hi:[1,0] neg_lo:[0,1] neg_hi:[0,1]\n"
        "v_pk_add_f32 v[36:37], v[36:37], v[18:19] op_sel_hi:[1,0] neg_lo:[0,1] neg_hi:[0,1]\n"
        "v_pk_add_f32 v[38:39], v[38:39], v[18:19] op_sel_hi:[1,0] neg_lo:[0,1] neg_hi:[0,1]\n"
        "v_cvt_pk_bf16_f32 v12, v32, v33\n"
        "v_cvt_pk_bf16_f32 v13, v34, v35\n"
        "v_cvt_pk_bf16_f32 v14, v36, v37\n"
        "v_cvt_pk_bf16_f32 v15, v38, v39\n"
        "v_lshlrev_b32 v32, 16, v12\n"
        "v_and_b32 v33, s3, v12\n"
        "v_lshlrev_b32 v34, 16, v13\n"
        "v_and_b32 v35, s3, v13\n"
        "v_lshlrev_b32 v36, 16, v14\n"
        "v_and_b32 v37, s3, v14\n"
        "v_lshlrev_b32 v38, 16, v15\n"
        "v_and_b32 v39, s3, v15\n"
        "v_pk_mul_f32 v[32:33], v[32:33], v[18:19] op_sel:[0,1] op_sel_hi:[1,1]\n"
        "v_pk_mul_f32 v[34:35], v[34:35], v[18:19] op_sel:[0,1] op_sel_hi:[1,1]\n"
        "v_pk_mul_f32 v[36:37], v[36:37], v[18:19] op_sel:[0,1] op_sel_hi:[1,1]\n"
        "v_pk_mul_f32 v[38:39], v[38:39], v[18:19] op_sel:[0,1] op_sel_hi:[1,1]\n"
        "v_cvt_pk_bf16_f32 v12, v32, v33\n"
        "v_cvt_pk_bf16_f32 v13, v34, v35\n"
        "v_cvt_pk_bf16_f32 v14, v36, v37\n"
        "v_cvt_pk_bf16_f32 v15, v38, v39\n"
        "ds_write_b128 v22, v[12:15] offset:1088\n"
        "s_cmp_eq_u32 s37, 2\n"
        "s_waitcnt lgkmcnt(0)\n"
        "s_barrier\n"
        "s_cbranch_scc1 .Ls4d_L_KEEP\n"
        "s_endpgm\n"
        ".Ls4d_L_KEEP:\n"
        "v_lshlrev_b32 v20, 7, v3\n"
        "v_add_u32 v20, 0x41000, v20\n"
        "v_add_u32 v21, 0x2000, v20\n"
        "s_mov_b32 s3, 1048576\n"
        ".Ls4d_L_APOLL:\n"
        "global_load_dwordx2 v[32:33], v20, s[28:29] sc1\n"
        "global_load_dwordx2 v[34:35], v21, s[28:29] sc1\n"
        "s_sub_u32 s3, s3, 1\n"
        "s_cbranch_scc1 .Ls4d_L_ADONE\n"
        "s_waitcnt vmcnt(0)\n"
        "v_cmp_ne_u32 vcc, s33, v33\n"
        "v_cmp_ne_u32 s[50:51], s33, v35\n"
        "s_nop 0\n"
        "s_or_b64 exec, vcc, s[50:51]\n"
        "s_sleep 4\n"
        "s_cbranch_execnz .Ls4d_L_APOLL\n"
        ".Ls4d_L_ADONE:\n"
        "s_mov_b64 exec, 1\n"
        "v_mov_b32 v40, 0\n"
        "global_store_dword v40, v45, s[28:29] sc1\n"
        "s_endpgm\n"
        ".Ls4d_L_WRK:\n"
        "v_and_b32 v6, 0xff, v0\n"
        "v_mov_b32 v4, 0x272c0\n"
        "v_lshlrev_b32 v5, 1, v6\n"
        "v_lshlrev_b32 v7, 3, v6\n"
        "v_add_u32 v7, 0x9c000, v7\n"
        "v_mov_b32 v26, 0x9d000\n"
        "s_movk_i32 s3, 0x7ff\n"
        "s_barrier\n"
        "ds_read_b128 v[8:11], v4\n"
        "s_waitcnt lgkmcnt(0)\n"
        "v_bfi_b32 v12, v8, 0, s3\n"
        "v_bfi_b32 v13, v10, 0, s3\n"
        "v_lshl_add_u32 v12, v12, 10, v5\n"
        "v_lshl_add_u32 v13, v13, 10, v5\n"
        "global_load_ushort v16, v12, s[4:5] offset:0\n"
        "global_load_ushort v18, v12, s[4:5] offset:512\n"
        "global_load_ushort v20, v13, s[4:5] offset:0\n"
        "global_load_ushort v22, v13, s[4:5] offset:512\n"
        "v_mov_b32 v17, v9\n"
        "v_mov_b32 v19, v9\n"
        "v_mov_b32 v21, v9\n"
        "v_mov_b32 v23, v9\n"
        "s_waitcnt vmcnt(0)\n"
        "global_store_dwordx2 v7, v[16:17], s[28:29] offset:-4096 sc1\n"
        "global_store_dwordx2 v7, v[18:19], s[28:29] offset:-2048 sc1\n"
        "global_store_dwordx2 v7, v[20:21], s[28:29] offset:0 sc1\n"
        "global_store_dwordx2 v7, v[22:23], s[28:29] offset:2048 sc1\n"
        "s_cmp_eq_u32 s30, 4\n"
        "s_cbranch_scc0 .Ls4d_L_SEL1\n"
        "s_mov_b64 exec, 1\n"
        "global_store_dwordx2 v26, v[8:9], s[28:29] sc1\n"
        "global_store_dwordx2 v26, v[10:11], s[28:29] offset:8 sc1\n"
        ".Ls4d_L_SEL1:\n"
        "s_endpgm\n"
        ".Ls4d_L_FFN:\n"
        "s_sub_u32 s40, s30, 4\n"
        "v_lshrrev_b32 v28, 2, v3\n"
        "v_and_b32 v29, 3, v0\n"
        "s_lshl_b32 s3, s40, 7\n"
        "v_add_u32 v28, s3, v28\n"
        "v_lshlrev_b32 v29, 4, v29\n"
        "v_and_b32 v30, 32, v0\n"
        "v_xor_b32 v29, v29, v30\n"
        "s_lshl_b32 s3, s38, 6\n"
        "v_mul_u32_u24 v28, 0xac0, v28\n"
        "v_add3_u32 v28, v28, v29, s3\n"
        "s_mul_i32 s39, s40, 24576\n"
        "v_lshrrev_b32 v20, 3, v3\n"
        "v_and_b32 v21, 7, v0\n"
        "v_lshlrev_b32 v21, 4, v21\n"
        "v_and_b32 v22, 16, v0\n"
        "v_lshlrev_b32 v22, 1, v22\n"
        "v_xor_b32 v21, v21, v22\n"
        "s_lshl_b32 s3, s38, 15\n"
        "s_lshl_b32 s41, s40, 8\n"
        "s_add_u32 s3, s3, s41\n"
        "v_lshl_add_u32 v20, v20, 10, v21\n"
        "v_add_u32 v20, s3, v20\n"
        "v_xor_b32 v23, 64, v20\n"
        "v_add_u32 v21, 0x2000, v23\n"
        "v_add_u32 v22, 0x4000, v20\n"
        "v_add_u32 v23, 0x6000, v23\n"
        "v_lshrrev_b32 v25, 4, v3\n"
        "v_bfe_u32 v1, v3, 2, 2\n"
        "v_lshl_add_u32 v25, v25, 3, v1\n"
        "v_and_b32 v1, 3, v0\n"
        "v_lshlrev_b32 v1, 3, v1\n"
        "v_add_u32 v1, s39, v1\n"
        "v_lshl_add_u32 v27, v25, 7, v1\n"
        "v_lshl_add_u32 v25, v25, 6, v1\n"
        "v_and_b32 v1, 16, v0\n"
        "v_lshl_add_u32 v36, v1, 1, v25\n"
        "v_xor_b32 v37, 32, v36\n"
        "v_and_b32 v1, 24, v0\n"
        "v_lshl_add_u32 v38, v1, 2, v27\n"
        "v_xor_b32 v39, 32, v38\n"
        "v_xor_b32 v40, 64, v38\n"
        "v_xor_b32 v41, 96, v38\n"
        "v_and_b32 v6, 48, v0\n"
        "v_bfe_u32 v4, v3, 3, 1\n"
        "v_mul_u32_u24 v4, 1088, v4\n"
        "s_add_u32 s3, s41, 98304\n"
        "v_add3_u32 v4, v4, v6, s3\n"
        "v_and_b32 v5, 1, v0\n"
        "v_lshl_add_u32 v5, v5, 6, v6\n"
        "v_lshlrev_b32 v9, 3, v3\n"
        "v_add_u32 v9, 0x18840, v9\n"
        "s_lshl_b32 s3, s40, 9\n"
        "v_add_u32 v8, s3, v9\n"
        "s_lshl_b32 s3, s40, 7\n"
        "s_add_u32 s3, s3, 102464\n"
        "v_lshl_add_u32 v10, v3, 1, s3\n"
        "v_add_u32 v11, s3, v5\n"
        "v_mov_b32 v13, 0x19400\n"
        "s_mov_b32 vcc_lo, 0xf000f\n"
        "s_mov_b32 vcc_hi, 0xf000f\n"
        "v_cndmask_b32 v14, v13, v11, vcc\n"
        "s_mov_b32 vcc_lo, 0xf000f0\n"
        "s_mov_b32 vcc_hi, 0xf000f0\n"
        "v_cndmask_b32 v15, v13, v11, vcc\n"
        "s_mov_b32 vcc_lo, 0xf000f00\n"
        "s_mov_b32 vcc_hi, 0xf000f00\n"
        "v_cndmask_b32 v16, v13, v11, vcc\n"
        "s_mov_b32 vcc_lo, 0xf000f000\n"
        "s_mov_b32 vcc_hi, 0xf000f000\n"
        "v_cndmask_b32 v17, v13, v11, vcc\n"
        "v_mov_b32 v32, 0\n"
        "v_mov_b32 v33, 0\n"
        "v_mov_b32 v34, 0\n"
        "v_mov_b32 v35, 0\n"
        "ds_write_b128 v13, v[32:35]\n"
        "s_lshl_b32 s3, s38, 13\n"
        "s_lshl_b32 s41, s40, 10\n"
        "s_add_u32 s3, s3, s41\n"
        "s_add_u32 s3, s3, 286720\n"
        "v_lshl_add_u32 v12, v3, 3, s3\n"
        "v_mov_b32 v7, 0x272c4\n"
        "s_mov_b32 vcc_lo, 0xffff0000\n"
        "s_mov_b32 vcc_hi, 0xffff0000\n"
        "s_barrier\n"
        "s_add_u32 m0, s39, 0\n"
        "s_nop 0\n"
        "global_load_lds_dwordx4 v28, s[20:21]\n"
        "s_add_u32 m0, s39, 8192\n"
        "s_nop 0\n"
        "global_load_lds_dwordx4 v28, s[22:23]\n"
        "v_add_u32 v28, 0xac00, v28\n"
        "s_add_u32 m0, s39, 1024\n"
        "s_nop 0\n"
        "global_load_lds_dwordx4 v28, s[20:21]\n"
        "s_add_u32 m0, s39, 9216\n"
        "s_nop 0\n"
        "global_load_lds_dwordx4 v28, s[22:23]\n"
        "v_add_u32 v28, 0xac00, v28\n"
        "s_add_u32 m0, s39, 2048\n"
        "s_nop 0\n"
        "global_load_lds_dwordx4 v28, s[20:21]\n"
        "s_add_u32 m0, s39, 10240\n"
        "s_nop 0\n"
        "global_load_lds_dwordx4 v28, s[22:23]\n"
        "v_add_u32 v28, 0xac00, v28\n"
        "s_add_u32 m0, s39, 3072\n"
        "s_nop 0\n"
        "global_load_lds_dwordx4 v28, s[20:21]\n"
        "s_add_u32 m0, s39, 11264\n"
        "s_nop 0\n"
        "global_load_lds_dwordx4 v28, s[22:23]\n"
        "v_add_u32 v28, 0xac00, v28\n"
        "s_add_u32 m0, s39, 4096\n"
        "s_nop 0\n"
        "global_load_lds_dwordx4 v28, s[20:21]\n"
        "s_add_u32 m0, s39, 12288\n"
        "s_nop 0\n"
        "global_load_lds_dwordx4 v28, s[22:23]\n"
        "v_add_u32 v28, 0xac00, v28\n"
        "s_add_u32 m0, s39, 5120\n"
        "s_nop 0\n"
        "global_load_lds_dwordx4 v28, s[20:21]\n"
        "s_add_u32 m0, s39, 13312\n"
        "s_nop 0\n"
        "global_load_lds_dwordx4 v28, s[22:23]\n"
        "v_add_u32 v28, 0xac00, v28\n"
        "s_add_u32 m0, s39, 6144\n"
        "s_nop 0\n"
        "global_load_lds_dwordx4 v28, s[20:21]\n"
        "s_add_u32 m0, s39, 14336\n"
        "s_nop 0\n"
        "global_load_lds_dwordx4 v28, s[22:23]\n"
        "v_add_u32 v28, 0xac00, v28\n"
        "s_add_u32 m0, s39, 7168\n"
        "s_nop 0\n"
        "global_load_lds_dwordx4 v28, s[20:21]\n"
        "s_add_u32 m0, s39, 15360\n"
        "s_nop 0\n"
        "global_load_lds_dwordx4 v28, s[22:23]\n"
        "s_waitcnt vmcnt(12)\n"
        "ds_read_b64_tr_b16 v[96:97], v36 offset:0\n"
        "ds_read_b64_tr_b16 v[98:99], v36 offset:256\n"
        "ds_read_b64_tr_b16 v[100:101], v37 offset:0\n"
        "ds_read_b64_tr_b16 v[102:103], v37 offset:256\n"
        "ds_read_b64_tr_b16 v[128:129], v36 offset:8192\n"
        "ds_read_b64_tr_b16 v[130:131], v36 offset:8448\n"
        "ds_read_b64_tr_b16 v[132:133], v37 offset:8192\n"
        "ds_read_b64_tr_b16 v[134:135], v37 offset:8448\n"
        "s_add_u32 m0, s39, 16384\n"
        "s_nop 0\n"
        "global_load_lds_dwordx4 v20, s[24:25] offset:0\n"
        "s_add_u32 m0, s39, 20352\n"
        "s_nop 0\n"
        "global_load_lds_dwordx4 v20, s[24:25] offset:128\n"
        "s_waitcnt vmcnt(10)\n"
        "ds_read_b64_tr_b16 v[104:105], v36 offset:2048\n"
        "ds_read_b64_tr_b16 v[106:107], v36 offset:2304\n"
        "ds_read_b64_tr_b16 v[108:109], v37 offset:2048\n"
        "ds_read_b64_tr_b16 v[110:111], v37 offset:2304\n"
        "ds_read_b64_tr_b16 v[136:137], v36 offset:10240\n"
        "ds_read_b64_tr_b16 v[138:139], v36 offset:10496\n"
        "ds_read_b64_tr_b16 v[140:141], v37 offset:10240\n"
        "ds_read_b64_tr_b16 v[142:143], v37 offset:10496\n"
        "s_add_u32 m0, s39, 17408\n"
        "s_nop 0\n"
        "global_load_lds_dwordx4 v21, s[24:25] offset:0\n"
        "s_add_u32 m0, s39, 21376\n"
        "s_nop 0\n"
        "global_load_lds_dwordx4 v21, s[24:25] offset:128\n"
        "s_waitcnt vmcnt(8)\n"
        "ds_read_b64_tr_b16 v[112:113], v36 offset:4096\n"
        "ds_read_b64_tr_b16 v[114:115], v36 offset:4352\n"
        "ds_read_b64_tr_b16 v[116:117], v37 offset:4096\n"
        "ds_read_b64_tr_b16 v[118:119], v37 offset:4352\n"
        "ds_read_b64_tr_b16 v[144:145], v36 offset:12288\n"
        "ds_read_b64_tr_b16 v[146:147], v36 offset:12544\n"
        "ds_read_b64_tr_b16 v[148:149], v37 offset:12288\n"
        "ds_read_b64_tr_b16 v[150:151], v37 offset:12544\n"
        "s_add_u32 m0, s39, 18432\n"
        "s_nop 0\n"
        "global_load_lds_dwordx4 v22, s[24:25] offset:0\n"
        "s_add_u32 m0, s39, 22400\n"
        "s_nop 0\n"
        "global_load_lds_dwordx4 v22, s[24:25] offset:128\n"
        "s_waitcnt vmcnt(6)\n"
        "ds_read_b64_tr_b16 v[120:121], v36 offset:6144\n"
        "ds_read_b64_tr_b16 v[122:123], v36 offset:6400\n"
        "ds_read_b64_tr_b16 v[124:125], v37 offset:6144\n"
        "ds_read_b64_tr_b16 v[126:127], v37 offset:6400\n"
        "ds_read_b64_tr_b16 v[152:153], v36 offset:14336\n"
        "ds_read_b64_tr_b16 v[154:155], v36 offset:14592\n"
        "ds_read_b64_tr_b16 v[156:157], v37 offset:14336\n"
        "ds_read_b64_tr_b16 v[158:159], v37 offset:14592\n"
        "s_add_u32 m0, s39, 19456\n"
        "s_nop 0\n"
        "global_load_lds_dwordx4 v23, s[24:25] offset:0\n"
        "s_add_u32 m0, s39, 23424\n"
        "s_nop 0\n"
        "global_load_lds_dwordx4 v23, s[24:25] offset:128\n"
        "s_barrier\n"
        "ds_read_b128 v[44:47], v4 offset:0\n"
        "ds_read_b128 v[48:51], v4 offset:64\n"
        "ds_read_b128 v[52:55], v4 offset:128\n"
        "ds_read_b128 v[56:59], v4 offset:192\n"
        "ds_read_b32 v61, v7\n"
        "s_waitcnt lgkmcnt(4)\n"
        "v_mfma_f32_16x16x32_bf16 v[64:67], v[44:47], v[96:99], 0\n"
        "v_mfma_f32_16x16x32_bf16 v[68:71], v[44:47], v[128:131], 0\n"
        "v_mfma_f32_16x16x32_bf16 v[72:75], v[44:47], v[100:103], 0\n"
        "v_mfma_f32_16x16x32_bf16 v[76:79], v[44:47], v[132:135], 0\n"
        "s_waitcnt lgkmcnt(3)\n"
        "v_mfma_f32_16x16x32_bf16 v[64:67], v[48:51], v[104:107], v[64:67]\n"
        "v_mfma_f32_16x16x32_bf16 v[68:71], v[48:51], v[136:139], v[68:71]\n"
        "v_mfma_f32_16x16x32_bf16 v[72:75], v[48:51], v[108:111], v[72:75]\n"
        "v_mfma_f32_16x16x32_bf16 v[76:79], v[48:51], v[140:143], v[76:79]\n"
        "s_waitcnt lgkmcnt(2)\n"
        "v_mfma_f32_16x16x32_bf16 v[64:67], v[52:55], v[112:115], v[64:67]\n"
        "v_mfma_f32_16x16x32_bf16 v[68:71], v[52:55], v[144:147], v[68:71]\n"
        "v_mfma_f32_16x16x32_bf16 v[72:75], v[52:55], v[116:119], v[72:75]\n"
        "v_mfma_f32_16x16x32_bf16 v[76:79], v[52:55], v[148:151], v[76:79]\n"
        "s_waitcnt lgkmcnt(1)\n"
        "v_mfma_f32_16x16x32_bf16 v[64:67], v[56:59], v[120:123], v[64:67]\n"
        "v_mfma_f32_16x16x32_bf16 v[68:71], v[56:59], v[152:155], v[68:71]\n"
        "v_mfma_f32_16x16x32_bf16 v[72:75], v[56:59], v[124:127], v[72:75]\n"
        "v_mfma_f32_16x16x32_bf16 v[76:79], v[56:59], v[156:159], v[76:79]\n"
        "s_nop 6\n"
        "v_cndmask_b32 v80, v64, v72, vcc\n"
        "v_cndmask_b32 v81, v68, v76, vcc\n"
        "ds_write_b64 v8, v[80:81]\n"
        "s_waitcnt lgkmcnt(0)\n"
        "s_barrier\n"
        "ds_read_b64 v[44:45], v9 offset:0\n"
        "ds_read_b64 v[46:47], v9 offset:512\n"
        "ds_read_b64 v[48:49], v9 offset:1024\n"
        "ds_read_b64 v[50:51], v9 offset:1536\n"
        "s_waitcnt vmcnt(0)\n"
        "ds_read_b64_tr_b16 v[160:161], v38 offset:16384\n"
        "ds_read_b64_tr_b16 v[162:163], v38 offset:16896\n"
        "ds_read_b64_tr_b16 v[164:165], v39 offset:16384\n"
        "ds_read_b64_tr_b16 v[166:167], v39 offset:16896\n"
        "v_mov_b32 v63, v61\n"
        "v_mov_b32 v65, v61\n"
        "v_mov_b32 v67, v61\n"
        "s_waitcnt lgkmcnt(6)\n"
        "v_pk_add_f32 v[44:45], v[44:45], v[46:47]\n"
        "s_waitcnt lgkmcnt(4)\n"
        "v_pk_add_f32 v[48:49], v[48:49], v[50:51]\n"
        "ds_read_b64_tr_b16 v[168:169], v40 offset:16384\n"
        "v_pk_add_f32 v[44:45], v[44:45], v[48:49]\n"
        "ds_read_b64_tr_b16 v[170:171], v40 offset:16896\n"
        "v_cvt_pk_bf16_f32 v44, v44, v45\n"
        "ds_read_b64_tr_b16 v[172:173], v41 offset:16384\n"
        "v_lshlrev_b32 v45, 16, v44\n"
        "ds_read_b64_tr_b16 v[174:175], v41 offset:16896\n"
        "v_mul_f32 v46, 0xbfb8aa3b, v45\n"
        "ds_read_b64_tr_b16 v[176:177], v38 offset:20480\n"
        "v_exp_f32 v46, v46\n"
        "ds_read_b64_tr_b16 v[178:179], v38 offset:20992\n"
        "v_and_b32 v44, 0xffff0000, v44\n"
        "v_add_f32 v46, 1.0, v46\n"
        "ds_read_b64_tr_b16 v[180:181], v39 offset:20480\n"
        "v_rcp_f32 v46, v46\n"
        "ds_read_b64_tr_b16 v[182:183], v39 offset:20992\n"
        "v_mul_f32 v45, v45, v46\n"
        "ds_read_b64_tr_b16 v[184:185], v40 offset:20480\n"
        "v_cvt_pk_bf16_f32 v45, v45, v45\n"
        "ds_read_b64_tr_b16 v[186:187], v40 offset:20992\n"
        "v_and_b32 v45, 0xffff0000, v45\n"
        "ds_read_b64_tr_b16 v[188:189], v41 offset:20480\n"
        "v_mul_f32 v44, v44, v45\n"
        "ds_read_b64_tr_b16 v[190:191], v41 offset:20992\n"
        "v_cvt_pk_bf16_f32 v44, v44, v44\n"
        "ds_write_b16 v10, v44\n"
        "ds_read_b128 v[44:47], v14\n"
        "ds_read_b128 v[48:51], v15\n"
        "ds_read_b128 v[52:55], v16\n"
        "ds_read_b128 v[56:59], v17\n"
        "s_waitcnt lgkmcnt(3)\n"
        "v_mfma_f32_16x16x32_bf16 v[72:75], v[44:47], v[160:163], 0\n"
        "s_waitcnt lgkmcnt(2)\n"
        "v_mfma_f32_16x16x32_bf16 v[72:75], v[48:51], v[164:167], v[72:75]\n"
        "s_waitcnt lgkmcnt(1)\n"
        "v_mfma_f32_16x16x32_bf16 v[72:75], v[52:55], v[168:171], v[72:75]\n"
        "s_waitcnt lgkmcnt(0)\n"
        "v_mfma_f32_16x16x32_bf16 v[72:75], v[56:59], v[172:175], v[72:75]\n"
        "v_mfma_f32_16x16x32_bf16 v[76:79], v[44:47], v[176:179], 0\n"
        "v_mfma_f32_16x16x32_bf16 v[76:79], v[48:51], v[180:183], v[76:79]\n"
        "v_mfma_f32_16x16x32_bf16 v[76:79], v[52:55], v[184:187], v[76:79]\n"
        "v_mfma_f32_16x16x32_bf16 v[76:79], v[56:59], v[188:191], v[76:79]\n"
        "s_nop 3\n"
        "v_mov_b32 v60, v72\n"
        "v_mov_b32 v62, v73\n"
        "global_store_dwordx2 v12, v[60:61], s[28:29] offset:-4096 sc1\n"
        "global_store_dwordx2 v12, v[62:63], s[28:29] sc1\n"
        "v_mov_b32 v64, v76\n"
        "v_mov_b32 v66, v77\n"
        "global_store_dwordx2 v12, v[64:65], s[28:29] offset:-3584 sc1\n"
        "global_store_dwordx2 v12, v[66:67], s[28:29] offset:512 sc1\n"
        "s_endpgm\n"
        ".Ls4d_L_E:\n"
        "v_add_u32 v74, 0x100000, v72\n"
        "v_add_u32 v75, 0x180000, v72\n"
        "v_and_b32 v7, 63, v0\n"
        "s_and_b32 s45, s30, 3\n"
        "s_mul_i32 s3, s45, 4608\n"
        "s_add_u32 s3, s3, 112320\n"
        "v_lshl_add_u32 v1, v7, 3, s3\n"
        "v_mov_b32 v4, 0\n"
        "v_mov_b32 v5, 0\n"
        "ds_write_b64 v1, v[4:5]\n"
        "s_lshl_b32 s3, s45, 9\n"
        "s_add_u32 m0, s3, 45760\n"
        "v_mov_b32 v4, 0x3c00\n"
        "ds_write_addtid_b32 v4\n"
        "ds_write_addtid_b32 v4 offset:256\n"
        "v_lshlrev_b32 v120, 2, v6\n"
        "v_add_u32 v120, 0x1b8c0, v120\n"
        "v_bfe_u32 v1, v6, 1, 2\n"
        "v_bfe_u32 v4, v6, 3, 4\n"
        "v_lshl_add_u32 v1, v1, 4, v4\n"
        "v_and_b32 v4, 1, v6\n"
        "v_mul_u32_u24 v4, 544, v4\n"
        "v_lshl_add_u32 v1, v1, 2, v4\n"
        "v_lshrrev_b32 v4, 7, v6\n"
        "v_mul_u32_u24 v4, 1088, v4\n"
        "v_add_u32 v121, v4, v1\n"
        "s_mul_i32 s3, s31, 16384\n"
        "v_lshl_add_u32 v122, v6, 4, s3\n"
        "v_add_u32 v123, 0x4000, v122\n"
        "v_add_u32 v122, 0x2000, v122\n"
        "s_add_u32 s26, s26, s36\n"
        "s_addc_u32 s27, s27, 0\n"
        "s_and_b32 s45, s30, 3\n"
        "s_lshl_b32 s3, s45, 4\n"
        "s_waitcnt lgkmcnt(0)\n"
        "s_lshr_b64 s[38:39], s[38:39], s3\n"
        "s_lshl_b32 s38, s38, 16\n"
        "s_add_u32 s33, s33, 1\n"
        "v_mov_b32 v140, 0x3fb8aa3b\n"
        "v_mul_f32 v140, s38, v140\n"
        "v_exp_f32 v140, v140\n"
        "s_nop 0\n"
        "v_cvt_pk_bf16_f32 v140, v140, v140\n"
        "v_and_b32 v140, 0xffff0000, v140\n"
        "v_mul_f32 v141, 0x3fa2f983, v140\n"
        "v_mul_f32 v140, 0xc138aa3b, v140\n"
        "s_waitcnt vmcnt(1)\n"
        "global_load_dwordx2 v[8:9], v72, s[4:5]\n"
        "global_load_dwordx2 v[10:11], v72, s[4:5] offset:1024\n"
        "global_load_dwordx2 v[12:13], v73, s[4:5]\n"
        "global_load_dwordx2 v[14:15], v73, s[4:5] offset:1024\n"
        "v_lshlrev_b32 v142, 16, v2\n"
        "v_and_b32 v143, 0xffff0000, v2\n"
        "v_mul_f32 v142, 0x3fb8aa3b, v142\n"
        "v_mul_f32 v143, 0x3fb8aa3b, v143\n"
        "v_exp_f32 v142, v142\n"
        "v_exp_f32 v143, v143\n"
        "v_lshlrev_b32 v144, 16, v3\n"
        "v_and_b32 v145, 0xffff0000, v3\n"
        "v_cvt_pk_bf16_f32 v143, v142, v143\n"
        "v_mul_f32 v144, v144, v141\n"
        "v_mul_f32 v145, v145, v141\n"
        "v_lshlrev_b32 v142, 16, v143\n"
        "v_and_b32 v143, 0xffff0000, v143\n"
        "v_fract_f32 v144, v144\n"
        "v_fract_f32 v145, v145\n"
        "v_mul_f32 v142, v142, v140\n"
        "v_mul_f32 v143, v143, v140\n"
        "v_cos_f32 v146, v144\n"
        "v_exp_f32 v142, v142\n"
        "v_exp_f32 v143, v143\n"
        "v_sin_f32 v147, v144\n"
        "v_cos_f32 v148, v145\n"
        "v_sin_f32 v149, v145\n"
        ".p2align 3\n"
        "v_pk_mul_f32 v[146:147], v[146:147], v[142:143] op_sel_hi:[1,0]\n"
        "v_pk_mul_f32 v[148:149], v[148:149], v[142:143] op_sel:[0,1] op_sel_hi:[1,1]\n"
        "s_setprio 1\n"
        "s_nop 0\n"
        "v_pk_mul_f32 v[154:155], v[146:147], v[146:147] op_sel:[1,1] op_sel_hi:[0,1] neg_lo:[1,0]\n"
        "v_pk_mul_f32 v[156:157], v[148:149], v[148:149] op_sel:[1,1] op_sel_hi:[0,1] neg_lo:[1,0]\n"
        "v_cvt_pk_f16_f32 v176, v146, v147\n"
        "v_cvt_pk_f16_f32 v177, v148, v149\n"
        "v_pk_fma_f32 v[150:151], v[146:147], v[146:147], v[154:155] op_sel_hi:[1,0,1]\n"
        "ds_write_addtid_b32 v176 offset:2080\n"
        "v_pk_fma_f32 v[152:153], v[148:149], v[148:149], v[156:157] op_sel_hi:[1,0,1]\n"
        "ds_write_addtid_b32 v177 offset:2336\n"
        "v_pk_mul_f32 v[154:155], v[146:147], v[150:151] op_sel:[1,1] op_sel_hi:[0,1] neg_lo:[1,0]\n"
        "v_pk_mul_f32 v[156:157], v[148:149], v[152:153] op_sel:[1,1] op_sel_hi:[0,1] neg_lo:[1,0]\n"
        "v_cvt_pk_f16_f32 v178, v150, v151\n"
        "v_cvt_pk_f16_f32 v179, v152, v153\n"
        "v_pk_fma_f32 v[150:151], v[146:147], v[150:151], v[154:155] op_sel_hi:[1,0,1]\n"
        "ds_write_addtid_b32 v178 offset:4160\n"
        "v_pk_fma_f32 v[152:153], v[148:149], v[152:153], v[156:157] op_sel_hi:[1,0,1]\n"
        "ds_write_addtid_b32 v179 offset:4416\n"
        "v_pk_mul_f32 v[154:155], v[146:147], v[150:151] op_sel:[1,1] op_sel_hi:[0,1] neg_lo:[1,0]\n"
        "v_pk_mul_f32 v[156:157], v[148:149], v[152:153] op_sel:[1,1] op_sel_hi:[0,1] neg_lo:[1,0]\n"
        "v_cvt_pk_f16_f32 v180, v150, v151\n"
        "v_cvt_pk_f16_f32 v181, v152, v153\n"
        "v_pk_fma_f32 v[150:151], v[146:147], v[150:151], v[154:155] op_sel_hi:[1,0,1]\n"
        "ds_write_addtid_b32 v180 offset:6240\n"
        "v_pk_fma_f32 v[152:153], v[148:149], v[152:153], v[156:157] op_sel_hi:[1,0,1]\n"
        "ds_write_addtid_b32 v181 offset:6496\n"
        "v_pk_mul_f32 v[154:155], v[146:147], v[150:151] op_sel:[1,1] op_sel_hi:[0,1] neg_lo:[1,0]\n"
        "v_pk_mul_f32 v[156:157], v[148:149], v[152:153] op_sel:[1,1] op_sel_hi:[0,1] neg_lo:[1,0]\n"
        "v_cvt_pk_f16_f32 v182, v150, v151\n"
        "v_cvt_pk_f16_f32 v183, v152, v153\n"
        "v_pk_fma_f32 v[150:151], v[146:147], v[150:151], v[154:155] op_sel_hi:[1,0,1]\n"
        "ds_write_addtid_b32 v182 offset:8320\n"
        "v_pk_fma_f32 v[152:153], v[148:149], v[152:153], v[156:157] op_sel_hi:[1,0,1]\n"
        "ds_write_addtid_b32 v183 offset:8576\n"
        "v_pk_mul_f32 v[154:155], v[146:147], v[150:151] op_sel:[1,1] op_sel_hi:[0,1] neg_lo:[1,0]\n"
        "v_pk_mul_f32 v[156:157], v[148:149], v[152:153] op_sel:[1,1] op_sel_hi:[0,1] neg_lo:[1,0]\n"
        "v_cvt_pk_f16_f32 v184, v150, v151\n"
        "v_cvt_pk_f16_f32 v185, v152, v153\n"
        "v_pk_fma_f32 v[150:151], v[146:147], v[150:151], v[154:155] op_sel_hi:[1,0,1]\n"
        "ds_write_addtid_b32 v184 offset:10400\n"
        "v_pk_fma_f32 v[152:153], v[148:149], v[152:153], v[156:157] op_sel_hi:[1,0,1]\n"
        "ds_write_addtid_b32 v185 offset:10656\n"
        "v_pk_mul_f32 v[154:155], v[146:147], v[150:151] op_sel:[1,1] op_sel_hi:[0,1] neg_lo:[1,0]\n"
        "v_pk_mul_f32 v[156:157], v[148:149], v[152:153] op_sel:[1,1] op_sel_hi:[0,1] neg_lo:[1,0]\n"
        "v_cvt_pk_f16_f32 v186, v150, v151\n"
        "v_cvt_pk_f16_f32 v187, v152, v153\n"
        "v_pk_fma_f32 v[150:151], v[146:147], v[150:151], v[154:155] op_sel_hi:[1,0,1]\n"
        "ds_write_addtid_b32 v186 offset:12480\n"
        "v_pk_fma_f32 v[152:153], v[148:149], v[152:153], v[156:157] op_sel_hi:[1,0,1]\n"
        "ds_write_addtid_b32 v187 offset:12736\n"
        "v_pk_mul_f32 v[154:155], v[146:147], v[150:151] op_sel:[1,1] op_sel_hi:[0,1] neg_lo:[1,0]\n"
        "v_pk_mul_f32 v[156:157], v[148:149], v[152:153] op_sel:[1,1] op_sel_hi:[0,1] neg_lo:[1,0]\n"
        "v_cvt_pk_f16_f32 v188, v150, v151\n"
        "v_cvt_pk_f16_f32 v189, v152, v153\n"
        "v_pk_fma_f32 v[150:151], v[146:147], v[150:151], v[154:155] op_sel_hi:[1,0,1]\n"
        "ds_write_addtid_b32 v188 offset:14560\n"
        "v_pk_fma_f32 v[152:153], v[148:149], v[152:153], v[156:157] op_sel_hi:[1,0,1]\n"
        "ds_write_addtid_b32 v189 offset:14816\n"
        "v_pk_mul_f32 v[154:155], v[150:151], v[150:151] op_sel:[1,1] op_sel_hi:[0,1] neg_lo:[1,0]\n"
        "v_pk_mul_f32 v[156:157], v[152:153], v[152:153] op_sel:[1,1] op_sel_hi:[0,1] neg_lo:[1,0]\n"
        "v_cvt_pk_f16_f32 v190, v150, v151\n"
        "v_cvt_pk_f16_f32 v191, v152, v153\n"
        "v_pk_fma_f32 v[146:147], v[150:151], v[150:151], v[154:155] op_sel_hi:[1,0,1]\n"
        "ds_write_addtid_b32 v190 offset:16640\n"
        "v_pk_fma_f32 v[148:149], v[152:153], v[152:153], v[156:157] op_sel_hi:[1,0,1]\n"
        "ds_write_addtid_b32 v191 offset:16896\n"
        "v_pk_mul_f16 v154, v190, v176 op_sel:[1,1] op_sel_hi:[0,1] neg_lo:[1,0]\n"
        "v_pk_mul_f16 v155, v191, v177 op_sel:[1,1] op_sel_hi:[0,1] neg_lo:[1,0]\n"
        "v_pk_fma_f16 v162, v190, v176, v154 op_sel_hi:[1,0,1]\n"
        "v_pk_fma_f16 v163, v191, v177, v155 op_sel_hi:[1,0,1]\n"
        "ds_write_addtid_b32 v162 offset:18720\n"
        "ds_write_addtid_b32 v163 offset:18976\n"
        "v_pk_mul_f16 v154, v190, v178 op_sel:[1,1] op_sel_hi:[0,1] neg_lo:[1,0]\n"
        "v_pk_mul_f16 v155, v191, v179 op_sel:[1,1] op_sel_hi:[0,1] neg_lo:[1,0]\n"
        "v_pk_fma_f16 v160, v190, v178, v154 op_sel_hi:[1,0,1]\n"
        "v_pk_fma_f16 v161, v191, v179, v155 op_sel_hi:[1,0,1]\n"
        "ds_write_addtid_b32 v160 offset:20800\n"
        "ds_write_addtid_b32 v161 offset:21056\n"
        "v_pk_mul_f16 v154, v190, v180 op_sel:[1,1] op_sel_hi:[0,1] neg_lo:[1,0]\n"
        "v_pk_mul_f16 v155, v191, v181 op_sel:[1,1] op_sel_hi:[0,1] neg_lo:[1,0]\n"
        "v_pk_fma_f16 v162, v190, v180, v154 op_sel_hi:[1,0,1]\n"
        "v_pk_fma_f16 v163, v191, v181, v155 op_sel_hi:[1,0,1]\n"
        "ds_write_addtid_b32 v162 offset:22880\n"
        "ds_write_addtid_b32 v163 offset:23136\n"
        "v_pk_mul_f16 v154, v190, v182 op_sel:[1,1] op_sel_hi:[0,1] neg_lo:[1,0]\n"
        "v_pk_mul_f16 v155, v191, v183 op_sel:[1,1] op_sel_hi:[0,1] neg_lo:[1,0]\n"
        "v_pk_fma_f16 v160, v190, v182, v154 op_sel_hi:[1,0,1]\n"
        "v_pk_fma_f16 v161, v191, v183, v155 op_sel_hi:[1,0,1]\n"
        "ds_write_addtid_b32 v160 offset:24960\n"
        "ds_write_addtid_b32 v161 offset:25216\n"
        "s_setprio 0\n"
        "s_nop 0\n"
        "v_pk_mul_f16 v154, v190, v184 op_sel:[1,1] op_sel_hi:[0,1] neg_lo:[1,0]\n"
        "v_pk_mul_f16 v155, v191, v185 op_sel:[1,1] op_sel_hi:[0,1] neg_lo:[1,0]\n"
        "v_pk_fma_f16 v162, v190, v184, v154 op_sel_hi:[1,0,1]\n"
        "v_pk_fma_f16 v163, v191, v185, v155 op_sel_hi:[1,0,1]\n"
        "ds_write_addtid_b32 v162 offset:27040\n"
        "ds_write_addtid_b32 v163 offset:27296\n"
        "v_pk_mul_f16 v154, v190, v186 op_sel:[1,1] op_sel_hi:[0,1] neg_lo:[1,0]\n"
        "v_pk_mul_f16 v155, v191, v187 op_sel:[1,1] op_sel_hi:[0,1] neg_lo:[1,0]\n"
        "v_pk_fma_f16 v160, v190, v186, v154 op_sel_hi:[1,0,1]\n"
        "v_pk_fma_f16 v161, v191, v187, v155 op_sel_hi:[1,0,1]\n"
        "ds_write_addtid_b32 v160 offset:29120\n"
        "ds_write_addtid_b32 v161 offset:29376\n"
        "v_pk_mul_f16 v154, v190, v188 op_sel:[1,1] op_sel_hi:[0,1] neg_lo:[1,0]\n"
        "v_pk_mul_f16 v155, v191, v189 op_sel:[1,1] op_sel_hi:[0,1] neg_lo:[1,0]\n"
        "v_pk_fma_f16 v162, v190, v188, v154 op_sel_hi:[1,0,1]\n"
        "v_pk_fma_f16 v163, v191, v189, v155 op_sel_hi:[1,0,1]\n"
        "ds_write_addtid_b32 v162 offset:31200\n"
        "ds_write_addtid_b32 v163 offset:31456\n"
        "v_pk_mul_f32 v[154:155], v[146:147], v[150:151] op_sel:[1,1] op_sel_hi:[0,1] neg_lo:[1,0]\n"
        "v_pk_mul_f32 v[156:157], v[148:149], v[152:153] op_sel:[1,1] op_sel_hi:[0,1] neg_lo:[1,0]\n"
        "v_cvt_pk_f16_f32 v192, v146, v147\n"
        "v_cvt_pk_f16_f32 v193, v148, v149\n"
        "v_pk_fma_f32 v[150:151], v[146:147], v[150:151], v[154:155] op_sel_hi:[1,0,1]\n"
        "ds_write_addtid_b32 v192 offset:33280\n"
        "v_pk_fma_f32 v[152:153], v[148:149], v[152:153], v[156:157] op_sel_hi:[1,0,1]\n"
        "ds_write_addtid_b32 v193 offset:33536\n"
        "v_pk_mul_f16 v154, v192, v176 op_sel:[1,1] op_sel_hi:[0,1] neg_lo:[1,0]\n"
        "v_pk_mul_f16 v155, v193, v177 op_sel:[1,1] op_sel_hi:[0,1] neg_lo:[1,0]\n"
        "v_pk_fma_f16 v162, v192, v176, v154 op_sel_hi:[1,0,1]\n"
        "v_pk_fma_f16 v163, v193, v177, v155 op_sel_hi:[1,0,1]\n"
        "ds_write_addtid_b32 v162 offset:35360\n"
        "ds_write_addtid_b32 v163 offset:35616\n"
        "v_pk_mul_f16 v154, v192, v178 op_sel:[1,1] op_sel_hi:[0,1] neg_lo:[1,0]\n"
        "v_pk_mul_f16 v155, v193, v179 op_sel:[1,1] op_sel_hi:[0,1] neg_lo:[1,0]\n"
        "v_pk_fma_f16 v160, v192, v178, v154 op_sel_hi:[1,0,1]\n"
        "v_pk_fma_f16 v161, v193, v179, v155 op_sel_hi:[1,0,1]\n"
        "ds_write_addtid_b32 v160 offset:37440\n"
        "ds_write_addtid_b32 v161 offset:37696\n"
        "v_pk_mul_f16 v154, v192, v180 op_sel:[1,1] op_sel_hi:[0,1] neg_lo:[1,0]\n"
        "v_pk_mul_f16 v155, v193, v181 op_sel:[1,1] op_sel_hi:[0,1] neg_lo:[1,0]\n"
        "v_pk_fma_f16 v162, v192, v180, v154 op_sel_hi:[1,0,1]\n"
        "v_pk_fma_f16 v163, v193, v181, v155 op_sel_hi:[1,0,1]\n"
        "ds_write_addtid_b32 v162 offset:39520\n"
        "ds_write_addtid_b32 v163 offset:39776\n"
        "v_pk_mul_f16 v154, v192, v182 op_sel:[1,1] op_sel_hi:[0,1] neg_lo:[1,0]\n"
        "v_pk_mul_f16 v155, v193, v183 op_sel:[1,1] op_sel_hi:[0,1] neg_lo:[1,0]\n"
        "v_pk_fma_f16 v160, v192, v182, v154 op_sel_hi:[1,0,1]\n"
        "v_pk_fma_f16 v161, v193, v183, v155 op_sel_hi:[1,0,1]\n"
        "ds_write_addtid_b32 v160 offset:41600\n"
        "ds_write_addtid_b32 v161 offset:41856\n"
        "v_pk_mul_f16 v154, v192, v184 op_sel:[1,1] op_sel_hi:[0,1] neg_lo:[1,0]\n"
        "v_pk_mul_f16 v155, v193, v185 op_sel:[1,1] op_sel_hi:[0,1] neg_lo:[1,0]\n"
        "v_pk_fma_f16 v162, v192, v184, v154 op_sel_hi:[1,0,1]\n"
        "v_pk_fma_f16 v163, v193, v185, v155 op_sel_hi:[1,0,1]\n"
        "ds_write_addtid_b32 v162 offset:43680\n"
        "ds_write_addtid_b32 v163 offset:43936\n"
        "v_pk_mul_f16 v154, v192, v186 op_sel:[1,1] op_sel_hi:[0,1] neg_lo:[1,0]\n"
        "v_pk_mul_f16 v155, v193, v187 op_sel:[1,1] op_sel_hi:[0,1] neg_lo:[1,0]\n"
        "v_pk_fma_f16 v160, v192, v186, v154 op_sel_hi:[1,0,1]\n"
        "v_pk_fma_f16 v161, v193, v187, v155 op_sel_hi:[1,0,1]\n"
        "ds_write_addtid_b32 v160 offset:45760\n"
        "ds_write_addtid_b32 v161 offset:46016\n"
        "v_pk_mul_f16 v154, v192, v188 op_sel:[1,1] op_sel_hi:[0,1] neg_lo:[1,0]\n"
        "v_pk_mul_f16 v155, v193, v189 op_sel:[1,1] op_sel_hi:[0,1] neg_lo:[1,0]\n"
        "v_pk_fma_f16 v162, v192, v188, v154 op_sel_hi:[1,0,1]\n"
        "v_pk_fma_f16 v163, v193, v189, v155 op_sel_hi:[1,0,1]\n"
        "ds_write_addtid_b32 v162 offset:47840\n"
        "ds_write_addtid_b32 v163 offset:48096\n"
        "v_cvt_pk_f16_f32 v194, v150, v151\n"
        "v_cvt_pk_f16_f32 v195, v152, v153\n"
        "ds_write_addtid_b32 v194 offset:49920\n"
        "ds_write_addtid_b32 v195 offset:50176\n"
        "v_pk_mul_f16 v154, v194, v176 op_sel:[1,1] op_sel_hi:[0,1] neg_lo:[1,0]\n"
        "v_pk_mul_f16 v155, v195, v177 op_sel:[1,1] op_sel_hi:[0,1] neg_lo:[1,0]\n"
        "v_pk_fma_f16 v162, v194, v176, v154 op_sel_hi:[1,0,1]\n"
        "v_pk_fma_f16 v163, v195, v177, v155 op_sel_hi:[1,0,1]\n"
        "ds_write_addtid_b32 v162 offset:52000\n"
        "ds_write_addtid_b32 v163 offset:52256\n"
        "v_pk_mul_f16 v154, v194, v178 op_sel:[1,1] op_sel_hi:[0,1] neg_lo:[1,0]\n"
        "v_pk_mul_f16 v155, v195, v179 op_sel:[1,1] op_sel_hi:[0,1] neg_lo:[1,0]\n"
        "v_pk_fma_f16 v160, v194, v178, v154 op_sel_hi:[1,0,1]\n"
        "v_pk_fma_f16 v161, v195, v179, v155 op_sel_hi:[1,0,1]\n"
        "ds_write_addtid_b32 v160 offset:54080\n"
        "ds_write_addtid_b32 v161 offset:54336\n"
        "v_pk_mul_f16 v154, v194, v180 op_sel:[1,1] op_sel_hi:[0,1] neg_lo:[1,0]\n"
        "v_pk_mul_f16 v155, v195, v181 op_sel:[1,1] op_sel_hi:[0,1] neg_lo:[1,0]\n"
        "v_pk_fma_f16 v162, v194, v180, v154 op_sel_hi:[1,0,1]\n"
        "v_pk_fma_f16 v163, v195, v181, v155 op_sel_hi:[1,0,1]\n"
        "ds_write_addtid_b32 v162 offset:56160\n"
        "ds_write_addtid_b32 v163 offset:56416\n"
        "v_pk_mul_f16 v154, v194, v182 op_sel:[1,1] op_sel_hi:[0,1] neg_lo:[1,0]\n"
        "v_pk_mul_f16 v155, v195, v183 op_sel:[1,1] op_sel_hi:[0,1] neg_lo:[1,0]\n"
        "v_pk_fma_f16 v160, v194, v182, v154 op_sel_hi:[1,0,1]\n"
        "v_pk_fma_f16 v161, v195, v183, v155 op_sel_hi:[1,0,1]\n"
        "ds_write_addtid_b32 v160 offset:58240\n"
        "ds_write_addtid_b32 v161 offset:58496\n"
        "v_pk_mul_f16 v154, v194, v184 op_sel:[1,1] op_sel_hi:[0,1] neg_lo:[1,0]\n"
        "v_pk_mul_f16 v155, v195, v185 op_sel:[1,1] op_sel_hi:[0,1] neg_lo:[1,0]\n"
        "v_pk_fma_f16 v162, v194, v184, v154 op_sel_hi:[1,0,1]\n"
        "v_pk_fma_f16 v163, v195, v185, v155 op_sel_hi:[1,0,1]\n"
        "ds_write_addtid_b32 v162 offset:60320\n"
        "ds_write_addtid_b32 v163 offset:60576\n"
        "v_pk_mul_f16 v154, v194, v186 op_sel:[1,1] op_sel_hi:[0,1] neg_lo:[1,0]\n"
        "v_pk_mul_f16 v155, v195, v187 op_sel:[1,1] op_sel_hi:[0,1] neg_lo:[1,0]\n"
        "v_pk_fma_f16 v160, v194, v186, v154 op_sel_hi:[1,0,1]\n"
        "v_pk_fma_f16 v161, v195, v187, v155 op_sel_hi:[1,0,1]\n"
        "ds_write_addtid_b32 v160 offset:62400\n"
        "ds_write_addtid_b32 v161 offset:62656\n"
        "v_pk_mul_f16 v154, v194, v188 op_sel:[1,1] op_sel_hi:[0,1] neg_lo:[1,0]\n"
        "v_pk_mul_f16 v155, v195, v189 op_sel:[1,1] op_sel_hi:[0,1] neg_lo:[1,0]\n"
        "v_pk_fma_f16 v162, v194, v188, v154 op_sel_hi:[1,0,1]\n"
        "v_pk_fma_f16 v163, v195, v189, v155 op_sel_hi:[1,0,1]\n"
        "ds_write_addtid_b32 v162 offset:64480\n"
        "ds_write_addtid_b32 v163 offset:64736\n"
        "s_waitcnt lgkmcnt(0)\n"
        "s_barrier\n"
        "global_load_dwordx4 v[24:27], v122, s[28:29] offset:-4096 sc1\n"
        "global_load_dwordx4 v[28:31], v122, s[28:29] offset:0 sc1\n"
        "global_load_dwordx4 v[32:35], v123, s[28:29] offset:-4096 sc1\n"
        "global_load_dwordx4 v[36:39], v123, s[28:29] offset:0 sc1\n"
        "v_mov_b32 v1, 0x272d0\n"
        "ds_read_b128 v[124:127], v1\n"
        "s_mov_b32 s46, 0xffff0000\n"
        "s_mov_b32 s47, 0xbf317218\n"
        "s_lshl_b32 s50, s40, 16\n"
        "s_and_b32 s51, s40, s46\n"
        "s_lshl_b32 s52, s41, 16\n"
        "s_and_b32 s53, s41, s46\n"
        "s_waitcnt lgkmcnt(0)\n"
        "v_rcp_f32 v124, v124\n"
        "v_rcp_f32 v125, v125\n"
        "v_rcp_f32 v126, v126\n"
        "v_rcp_f32 v127, v127\n"
        "v_mul_f32 v128, s50, v124\n"
        "v_mul_f32 v129, s51, v125\n"
        "v_mul_f32 v130, s52, v126\n"
        "v_mul_f32 v131, s53, v127\n"
        "v_mul_f32 v124, 0x40053526, v124\n"
        "v_mul_f32 v125, 0x40053526, v125\n"
        "v_mul_f32 v126, 0x40053526, v126\n"
        "v_mul_f32 v127, 0x40053526, v127\n"
        "s_waitcnt vmcnt(4)\n"
        "v_lshlrev_b32 v40, 16, v8\n"
        "v_and_b32 v41, s46, v8\n"
        "v_lshlrev_b32 v42, 16, v9\n"
        "v_and_b32 v43, s46, v9\n"
        "v_lshlrev_b32 v44, 16, v10\n"
        "v_and_b32 v45, s46, v10\n"
        "v_lshlrev_b32 v46, 16, v11\n"
        "v_and_b32 v47, s46, v11\n"
        "v_lshlrev_b32 v48, 16, v12\n"
        "v_and_b32 v49, s46, v12\n"
        "v_lshlrev_b32 v50, 16, v13\n"
        "v_and_b32 v51, s46, v13\n"
        "v_lshlrev_b32 v52, 16, v14\n"
        "v_and_b32 v53, s46, v14\n"
        "v_lshlrev_b32 v54, 16, v15\n"
        "v_and_b32 v55, s46, v15\n"
        "s_waitcnt vmcnt(0)\n"
        "v_cmp_ne_u32 vcc, s33, v25\n"
        "v_cmp_ne_u32 s[50:51], s33, v27\n"
        "v_cmp_ne_u32 s[52:53], s33, v29\n"
        "v_cmp_ne_u32 s[54:55], s33, v31\n"
        "v_cmp_ne_u32 s[56:57], s33, v33\n"
        "v_cmp_ne_u32 s[58:59], s33, v35\n"
        "v_cmp_ne_u32 s[60:61], s33, v37\n"
        "v_cmp_ne_u32 s[62:63], s33, v39\n"
        "s_or_b64 vcc, vcc, s[50:51]\n"
        "s_or_b64 s[52:53], s[52:53], s[54:55]\n"
        "s_or_b64 s[56:57], s[56:57], s[58:59]\n"
        "s_or_b64 s[60:61], s[60:61], s[62:63]\n"
        "s_or_b64 vcc, vcc, s[52:53]\n"
        "s_or_b64 s[56:57], s[56:57], s[60:61]\n"
        "s_or_b64 vcc, vcc, s[56:57]\n"
        "s_cbranch_vccnz .Ls4d_L_ERETRY\n"
        ".Ls4d_L_EGO:\n"
        "v_lshlrev_b32 v140, 16, v24\n"
        "v_lshlrev_b32 v141, 16, v26\n"
        "v_mul_f32 v140, s47, v140\n"
        "v_mul_f32 v141, s47, v141\n"
        "v_rcp_f32 v140, v140\n"
        "v_rcp_f32 v141, v141\n"
        "v_and_b32 v142, s46, v24\n"
        "v_and_b32 v143, s46, v26\n"
        "v_mul_f32 v142, v142, v140\n"
        "v_mul_f32 v143, v143, v141\n"
        "v_fma_f32 v80, v40, v140, -v142\n"
        "v_fma_f32 v84, v44, v141, -v143\n"
        "v_fma_f32 v81, v41, v140, -v142\n"
        "v_fma_f32 v85, v45, v141, -v143\n"
        "v_cvt_pk_f16_f32 v144, v80, v84\n"
        "v_cvt_pk_f16_f32 v145, v81, v85\n"
        "ds_write2st64_b32 v120, v144, v145 offset0:0 offset1:18\n"
        "v_fma_f32 v82, v42, v140, -v142\n"
        "v_fma_f32 v86, v46, v141, -v143\n"
        "v_fma_f32 v83, v43, v140, -v142\n"
        "v_fma_f32 v87, v47, v141, -v143\n"
        "v_cvt_pk_f16_f32 v146, v82, v86\n"
        "v_cvt_pk_f16_f32 v147, v83, v87\n"
        "ds_write2st64_b32 v120, v146, v147 offset0:36 offset1:54\n"
        "s_cmp_ge_u32 s30, 6\n"
        "global_load_dwordx2 v[16:17], v74, s[4:5]\n"
        "global_load_dwordx2 v[18:19], v74, s[4:5] offset:1024\n"
        "global_load_dwordx2 v[20:21], v75, s[4:5]\n"
        "global_load_dwordx2 v[22:23], v75, s[4:5] offset:1024\n"
        "s_waitcnt lgkmcnt(0)\n"
        "s_barrier\n"
        "s_cbranch_scc0 .Ls4d_L_E1\n"
        "s_barrier\n"
        ".Ls4d_L_E1:\n"
        "v_lshlrev_b32 v140, 16, v28\n"
        "v_lshlrev_b32 v141, 16, v30\n"
        "v_mul_f32 v140, s47, v140\n"
        "v_mul_f32 v141, s47, v141\n"
        "v_rcp_f32 v140, v140\n"
        "v_rcp_f32 v141, v141\n"
        "v_and_b32 v142, s46, v28\n"
        "v_and_b32 v143, s46, v30\n"
        "v_mul_f32 v142, v142, v140\n"
        "v_mul_f32 v143, v143, v141\n"
        "v_fma_f32 v88, v48, v140, -v142\n"
        "v_fma_f32 v92, v52, v141, -v143\n"
        "v_fma_f32 v89, v49, v140, -v142\n"
        "v_fma_f32 v93, v53, v141, -v143\n"
        "v_cvt_pk_f16_f32 v144, v88, v92\n"
        "v_cvt_pk_f16_f32 v145, v89, v93\n"
        "ds_write2st64_b32 v120, v144, v145 offset0:4 offset1:22\n"
        "v_fma_f32 v90, v50, v140, -v142\n"
        "v_fma_f32 v94, v54, v141, -v143\n"
        "v_fma_f32 v91, v51, v140, -v142\n"
        "v_fma_f32 v95, v55, v141, -v143\n"
        "v_cvt_pk_f16_f32 v146, v90, v94\n"
        "v_cvt_pk_f16_f32 v147, v91, v95\n"
        "ds_write2st64_b32 v120, v146, v147 offset0:40 offset1:58\n"
        "v_exp_f32 v112, v80\n"
        "v_exp_f32 v113, v81\n"
        "v_exp_f32 v114, v82\n"
        "v_exp_f32 v115, v83\n"
        "v_fma_f32 v112, v112, v124, v124\n"
        "v_fma_f32 v113, v113, v125, v125\n"
        "v_fma_f32 v114, v114, v126, v126\n"
        "v_fma_f32 v115, v115, v127, v127\n"
        "v_rcp_f32 v112, v112\n"
        "v_rcp_f32 v113, v113\n"
        "v_rcp_f32 v114, v114\n"
        "v_rcp_f32 v115, v115\n"
        "v_mul_f32 v112, v112, v80\n"
        "v_mul_f32 v113, v113, v81\n"
        "v_mul_f32 v114, v114, v82\n"
        "v_mul_f32 v115, v115, v83\n"
        "s_waitcnt lgkmcnt(0)\n"
        "s_barrier\n"
        "ds_read_b32 v132, v121 offset:0\n"
        "ds_read_b32 v134, v121 offset:8704\n"
        "ds_read_b32 v136, v121 offset:17408\n"
        "ds_read_b32 v138, v121 offset:26112\n"
        "ds_read_b32 v133, v121 offset:272\n"
        "ds_read_b32 v135, v121 offset:8976\n"
        "ds_read_b32 v137, v121 offset:17680\n"
        "ds_read_b32 v139, v121 offset:26384\n"
        "s_waitcnt lgkmcnt(7)\n"
        "v_fmac_f32 v132, v80, v128\n"
        "s_waitcnt lgkmcnt(6)\n"
        "v_fmac_f32 v134, v81, v129\n"
        "s_waitcnt lgkmcnt(5)\n"
        "v_fmac_f32 v136, v82, v130\n"
        "s_waitcnt lgkmcnt(4)\n"
        "v_fmac_f32 v138, v83, v131\n"
        "v_fmac_f32 v40, v132, v112\n"
        "v_fmac_f32 v41, v134, v113\n"
        "v_fmac_f32 v42, v136, v114\n"
        "v_fmac_f32 v43, v138, v115\n"
        "v_cvt_pk_bf16_f32 v8, v40, v41\n"
        "v_cvt_pk_bf16_f32 v9, v42, v43\n"
        "global_store_dwordx2 v72, v[8:9], s[26:27] offset:0\n"
        "v_exp_f32 v116, v84\n"
        "v_exp_f32 v117, v85\n"
        "v_exp_f32 v118, v86\n"
        "v_exp_f32 v119, v87\n"
        "v_fma_f32 v116, v116, v124, v124\n"
        "v_fma_f32 v117, v117, v125, v125\n"
        "v_fma_f32 v118, v118, v126, v126\n"
        "v_fma_f32 v119, v119, v127, v127\n"
        "v_rcp_f32 v116, v116\n"
        "v_rcp_f32 v117, v117\n"
        "v_rcp_f32 v118, v118\n"
        "v_rcp_f32 v119, v119\n"
        "v_mul_f32 v116, v116, v84\n"
        "v_mul_f32 v117, v117, v85\n"
        "v_mul_f32 v118, v118, v86\n"
        "v_mul_f32 v119, v119, v87\n"
        "s_waitcnt lgkmcnt(0)\n"
        "v_fmac_f32 v133, v84, v128\n"
        "v_fmac_f32 v135, v85, v129\n"
        "v_fmac_f32 v137, v86, v130\n"
        "v_fmac_f32 v139, v87, v131\n"
        "v_fmac_f32 v44, v133, v116\n"
        "v_fmac_f32 v45, v135, v117\n"
        "v_fmac_f32 v46, v137, v118\n"
        "v_fmac_f32 v47, v139, v119\n"
        "v_cvt_pk_bf16_f32 v10, v44, v45\n"
        "v_cvt_pk_bf16_f32 v11, v46, v47\n"
        "global_store_dwordx2 v72, v[10:11], s[26:27] offset:1024\n"
        "s_barrier\n"
        "s_waitcnt vmcnt(4)\n"
        "v_lshlrev_b32 v56, 16, v16\n"
        "v_and_b32 v57, s46, v16\n"
        "v_lshlrev_b32 v58, 16, v17\n"
        "v_and_b32 v59, s46, v17\n"
        "v_lshlrev_b32 v60, 16, v18\n"
        "v_and_b32 v61, s46, v18\n"
        "v_lshlrev_b32 v62, 16, v19\n"
        "v_and_b32 v63, s46, v19\n"
        "v_lshlrev_b32 v140, 16, v32\n"
        "v_lshlrev_b32 v141, 16, v34\n"
        "v_mul_f32 v140, s47, v140\n"
        "v_mul_f32 v141, s47, v141\n"
        "v_rcp_f32 v140, v140\n"
        "v_rcp_f32 v141, v141\n"
        "v_and_b32 v142, s46, v32\n"
        "v_and_b32 v143, s46, v34\n"
        "v_mul_f32 v142, v142, v140\n"
        "v_mul_f32 v143, v143, v141\n"
        "v_fma_f32 v96, v56, v140, -v142\n"
        "v_fma_f32 v100, v60, v141, -v143\n"
        "v_fma_f32 v97, v57, v140, -v142\n"
        "v_fma_f32 v101, v61, v141, -v143\n"
        "v_cvt_pk_f16_f32 v144, v96, v100\n"
        "v_cvt_pk_f16_f32 v145, v97, v101\n"
        "ds_write2st64_b32 v120, v144, v145 offset0:8 offset1:26\n"
        "v_fma_f32 v98, v58, v140, -v142\n"
        "v_fma_f32 v102, v62, v141, -v143\n"
        "v_fma_f32 v99, v59, v140, -v142\n"
        "v_fma_f32 v103, v63, v141, -v143\n"
        "v_cvt_pk_f16_f32 v146, v98, v102\n"
        "v_cvt_pk_f16_f32 v147, v99, v103\n"
        "ds_write2st64_b32 v120, v146, v147 offset0:44 offset1:62\n"
        "v_exp_f32 v112, v88\n"
        "v_exp_f32 v113, v89\n"
        "v_exp_f32 v114, v90\n"
        "v_exp_f32 v115, v91\n"
        "v_fma_f32 v112, v112, v124, v124\n"
        "v_fma_f32 v113, v113, v125, v125\n"
        "v_fma_f32 v114, v114, v126, v126\n"
        "v_fma_f32 v115, v115, v127, v127\n"
        "v_rcp_f32 v112, v112\n"
        "v_rcp_f32 v113, v113\n"
        "v_rcp_f32 v114, v114\n"
        "v_rcp_f32 v115, v115\n"
        "v_mul_f32 v112, v112, v88\n"
        "v_mul_f32 v113, v113, v89\n"
        "v_mul_f32 v114, v114, v90\n"
        "v_mul_f32 v115, v115, v91\n"
        "s_waitcnt lgkmcnt(0)\n"
        "s_barrier\n"
        "ds_read_b32 v132, v121 offset:2176\n"
        "ds_read_b32 v134, v121 offset:10880\n"
        "ds_read_b32 v136, v121 offset:19584\n"
        "ds_read_b32 v138, v121 offset:28288\n"
        "ds_read_b32 v133, v121 offset:2448\n"
        "ds_read_b32 v135, v121 offset:11152\n"
        "ds_read_b32 v137, v121 offset:19856\n"
        "ds_read_b32 v139, v121 offset:28560\n"
        "s_waitcnt lgkmcnt(7)\n"
        "v_fmac_f32 v132, v88, v128\n"
        "s_waitcnt lgkmcnt(6)\n"
        "v_fmac_f32 v134, v89, v129\n"
        "s_waitcnt lgkmcnt(5)\n"
        "v_fmac_f32 v136, v90, v130\n"
        "s_waitcnt lgkmcnt(4)\n"
        "v_fmac_f32 v138, v91, v131\n"
        "v_fmac_f32 v48, v132, v112\n"
        "v_fmac_f32 v49, v134, v113\n"
        "v_fmac_f32 v50, v136, v114\n"
        "v_fmac_f32 v51, v138, v115\n"
        "v_cvt_pk_bf16_f32 v12, v48, v49\n"
        "v_cvt_pk_bf16_f32 v13, v50, v51\n"
        "global_store_dwordx2 v73, v[12:13], s[26:27] offset:0\n"
        "v_exp_f32 v116, v92\n"
        "v_exp_f32 v117, v93\n"
        "v_exp_f32 v118, v94\n"
        "v_exp_f32 v119, v95\n"
        "v_fma_f32 v116, v116, v124, v124\n"
        "v_fma_f32 v117, v117, v125, v125\n"
        "v_fma_f32 v118, v118, v126, v126\n"
        "v_fma_f32 v119, v119, v127, v127\n"
        "v_rcp_f32 v116, v116\n"
        "v_rcp_f32 v117, v117\n"
        "v_rcp_f32 v118, v118\n"
        "v_rcp_f32 v119, v119\n"
        "v_mul_f32 v116, v116, v92\n"
        "v_mul_f32 v117, v117, v93\n"
        "v_mul_f32 v118, v118, v94\n"
        "v_mul_f32 v119, v119, v95\n"
        "s_waitcnt lgkmcnt(0)\n"
        "v_fmac_f32 v133, v92, v128\n"
        "v_fmac_f32 v135, v93, v129\n"
        "v_fmac_f32 v137, v94, v130\n"
        "v_fmac_f32 v139, v95, v131\n"
        "v_fmac_f32 v52, v133, v116\n"
        "v_fmac_f32 v53, v135, v117\n"
        "v_fmac_f32 v54, v137, v118\n"
        "v_fmac_f32 v55, v139, v119\n"
        "v_cvt_pk_bf16_f32 v14, v52, v53\n"
        "v_cvt_pk_bf16_f32 v15, v54, v55\n"
        "global_store_dwordx2 v73, v[14:15], s[26:27] offset:1024\n"
        "s_barrier\n"
        "s_waitcnt vmcnt(4)\n"
        "v_lshlrev_b32 v64, 16, v20\n"
        "v_and_b32 v65, s46, v20\n"
        "v_lshlrev_b32 v66, 16, v21\n"
        "v_and_b32 v67, s46, v21\n"
        "v_lshlrev_b32 v68, 16, v22\n"
        "v_and_b32 v69, s46, v22\n"
        "v_lshlrev_b32 v70, 16, v23\n"
        "v_and_b32 v71, s46, v23\n"
        "v_lshlrev_b32 v140, 16, v36\n"
        "v_lshlrev_b32 v141, 16, v38\n"
        "v_mul_f32 v140, s47, v140\n"
        "v_mul_f32 v141, s47, v141\n"
        "v_rcp_f32 v140, v140\n"
        "v_rcp_f32 v141, v141\n"
        "v_and_b32 v142, s46, v36\n"
        "v_and_b32 v143, s46, v38\n"
        "v_mul_f32 v142, v142, v140\n"
        "v_mul_f32 v143, v143, v141\n"
        "v_fma_f32 v104, v64, v140, -v142\n"
        "v_fma_f32 v108, v68, v141, -v143\n"
        "v_fma_f32 v105, v65, v140, -v142\n"
        "v_fma_f32 v109, v69, v141, -v143\n"
        "v_cvt_pk_f16_f32 v144, v104, v108\n"
        "v_cvt_pk_f16_f32 v145, v105, v109\n"
        "ds_write2st64_b32 v120, v144, v145 offset0:12 offset1:30\n"
        "v_fma_f32 v106, v66, v140, -v142\n"
        "v_fma_f32 v110, v70, v141, -v143\n"
        "v_fma_f32 v107, v67, v140, -v142\n"
        "v_fma_f32 v111, v71, v141, -v143\n"
        "v_cvt_pk_f16_f32 v146, v106, v110\n"
        "v_cvt_pk_f16_f32 v147, v107, v111\n"
        "ds_write2st64_b32 v120, v146, v147 offset0:48 offset1:66\n"
        "v_exp_f32 v112, v96\n"
        "v_exp_f32 v113, v97\n"
        "v_exp_f32 v114, v98\n"
        "v_exp_f32 v115, v99\n"
        "v_fma_f32 v112, v112, v124, v124\n"
        "v_fma_f32 v113, v113, v125, v125\n"
        "v_fma_f32 v114, v114, v126, v126\n"
        "v_fma_f32 v115, v115, v127, v127\n"
        "v_rcp_f32 v112, v112\n"
        "v_rcp_f32 v113, v113\n"
        "v_rcp_f32 v114, v114\n"
        "v_rcp_f32 v115, v115\n"
        "v_mul_f32 v112, v112, v96\n"
        "v_mul_f32 v113, v113, v97\n"
        "v_mul_f32 v114, v114, v98\n"
        "v_mul_f32 v115, v115, v99\n"
        "s_waitcnt lgkmcnt(0)\n"
        "s_barrier\n"
        "ds_read_b32 v132, v121 offset:4352\n"
        "ds_read_b32 v134, v121 offset:13056\n"
        "ds_read_b32 v136, v121 offset:21760\n"
        "ds_read_b32 v138, v121 offset:30464\n"
        "ds_read_b32 v133, v121 offset:4624\n"
        "ds_read_b32 v135, v121 offset:13328\n"
        "ds_read_b32 v137, v121 offset:22032\n"
        "ds_read_b32 v139, v121 offset:30736\n"
        "s_waitcnt lgkmcnt(7)\n"
        "v_fmac_f32 v132, v96, v128\n"
        "s_waitcnt lgkmcnt(6)\n"
        "v_fmac_f32 v134, v97, v129\n"
        "s_waitcnt lgkmcnt(5)\n"
        "v_fmac_f32 v136, v98, v130\n"
        "s_waitcnt lgkmcnt(4)\n"
        "v_fmac_f32 v138, v99, v131\n"
        "v_fmac_f32 v56, v132, v112\n"
        "v_fmac_f32 v57, v134, v113\n"
        "v_fmac_f32 v58, v136, v114\n"
        "v_fmac_f32 v59, v138, v115\n"
        "v_cvt_pk_bf16_f32 v16, v56, v57\n"
        "v_cvt_pk_bf16_f32 v17, v58, v59\n"
        "global_store_dwordx2 v74, v[16:17], s[26:27] offset:0\n"
        "v_exp_f32 v116, v100\n"
        "v_exp_f32 v117, v101\n"
        "v_exp_f32 v118, v102\n"
        "v_exp_f32 v119, v103\n"
        "v_fma_f32 v116, v116, v124, v124\n"
        "v_fma_f32 v117, v117, v125, v125\n"
        "v_fma_f32 v118, v118, v126, v126\n"
        "v_fma_f32 v119, v119, v127, v127\n"
        "v_rcp_f32 v116, v116\n"
        "v_rcp_f32 v117, v117\n"
        "v_rcp_f32 v118, v118\n"
        "v_rcp_f32 v119, v119\n"
        "v_mul_f32 v116, v116, v100\n"
        "v_mul_f32 v117, v117, v101\n"
        "v_mul_f32 v118, v118, v102\n"
        "v_mul_f32 v119, v119, v103\n"
        "s_waitcnt lgkmcnt(0)\n"
        "v_fmac_f32 v133, v100, v128\n"
        "v_fmac_f32 v135, v101, v129\n"
        "v_fmac_f32 v137, v102, v130\n"
        "v_fmac_f32 v139, v103, v131\n"
        "v_fmac_f32 v60, v133, v116\n"
        "v_fmac_f32 v61, v135, v117\n"
        "v_fmac_f32 v62, v137, v118\n"
        "v_fmac_f32 v63, v139, v119\n"
        "v_cvt_pk_bf16_f32 v18, v60, v61\n"
        "v_cvt_pk_bf16_f32 v19, v62, v63\n"
        "global_store_dwordx2 v74, v[18:19], s[26:27] offset:1024\n"
        "s_barrier\n"
        "v_exp_f32 v112, v104\n"
        "v_exp_f32 v113, v105\n"
        "v_exp_f32 v114, v106\n"
        "v_exp_f32 v115, v107\n"
        "v_fma_f32 v112, v112, v124, v124\n"
        "v_fma_f32 v113, v113, v125, v125\n"
        "v_fma_f32 v114, v114, v126, v126\n"
        "v_fma_f32 v115, v115, v127, v127\n"
        "v_rcp_f32 v112, v112\n"
        "v_rcp_f32 v113, v113\n"
        "v_rcp_f32 v114, v114\n"
        "v_rcp_f32 v115, v115\n"
        "v_mul_f32 v112, v112, v104\n"
        "v_mul_f32 v113, v113, v105\n"
        "v_mul_f32 v114, v114, v106\n"
        "v_mul_f32 v115, v115, v107\n"
        "v_exp_f32 v116, v108\n"
        "v_exp_f32 v117, v109\n"
        "v_exp_f32 v118, v110\n"
        "v_exp_f32 v119, v111\n"
        "v_fma_f32 v116, v116, v124, v124\n"
        "v_fma_f32 v117, v117, v125, v125\n"
        "v_fma_f32 v118, v118, v126, v126\n"
        "v_fma_f32 v119, v119, v127, v127\n"
        "v_rcp_f32 v116, v116\n"
        "v_rcp_f32 v117, v117\n"
        "v_rcp_f32 v118, v118\n"
        "v_rcp_f32 v119, v119\n"
        "v_mul_f32 v116, v116, v108\n"
        "v_mul_f32 v117, v117, v109\n"
        "v_mul_f32 v118, v118, v110\n"
        "v_mul_f32 v119, v119, v111\n"
        "s_waitcnt lgkmcnt(0)\n"
        "s_barrier\n"
        "ds_read_b32 v132, v121 offset:6528\n"
        "ds_read_b32 v134, v121 offset:15232\n"
        "ds_read_b32 v136, v121 offset:23936\n"
        "ds_read_b32 v138, v121 offset:32640\n"
        "ds_read_b32 v133, v121 offset:6800\n"
        "ds_read_b32 v135, v121 offset:15504\n"
        "ds_read_b32 v137, v121 offset:24208\n"
        "ds_read_b32 v139, v121 offset:32912\n"
        "s_waitcnt lgkmcnt(7)\n"
        "v_fmac_f32 v132, v104, v128\n"
        "s_waitcnt lgkmcnt(6)\n"
        "v_fmac_f32 v134, v105, v129\n"
        "s_waitcnt lgkmcnt(5)\n"
        "v_fmac_f32 v136, v106, v130\n"
        "s_waitcnt lgkmcnt(4)\n"
        "v_fmac_f32 v138, v107, v131\n"
        "v_fmac_f32 v64, v132, v112\n"
        "v_fmac_f32 v65, v134, v113\n"
        "v_fmac_f32 v66, v136, v114\n"
        "v_fmac_f32 v67, v138, v115\n"
        "v_cvt_pk_bf16_f32 v20, v64, v65\n"
        "v_cvt_pk_bf16_f32 v21, v66, v67\n"
        "global_store_dwordx2 v75, v[20:21], s[26:27] offset:0\n"
        "s_waitcnt lgkmcnt(0)\n"
        "v_fmac_f32 v133, v108, v128\n"
        "v_fmac_f32 v135, v109, v129\n"
        "v_fmac_f32 v137, v110, v130\n"
        "v_fmac_f32 v139, v111, v131\n"
        "v_fmac_f32 v68, v133, v116\n"
        "v_fmac_f32 v69, v135, v117\n"
        "v_fmac_f32 v70, v137, v118\n"
        "v_fmac_f32 v71, v139, v119\n"
        "v_cvt_pk_bf16_f32 v22, v68, v69\n"
        "v_cvt_pk_bf16_f32 v23, v70, v71\n"
        "global_store_dwordx2 v75, v[22:23], s[26:27] offset:1024\n"
        "v_mov_b32 v1, 0x272c0\n"
        "s_movk_i32 s3, 0x7ff\n"
        "s_barrier\n"
        "s_cbranch_scc1 .Ls4d_L_E2\n"
        "s_barrier\n"
        ".Ls4d_L_E2:\n"
        "ds_read_b64 v[2:3], v1\n"
        "ds_read_b128 v[24:27], v1 offset:32\n"
        "s_waitcnt lgkmcnt(1)\n"
        "v_bfi_b32 v2, v2, 0, s3\n"
        "v_bfi_b32 v3, v3, 0, s3\n"
        "v_bfe_u32 v4, v2, 1, 8\n"
        "v_bfe_u32 v5, v3, 1, 8\n"
        "v_cmp_eq_u32 vcc, v4, v6\n"
        "v_cmp_eq_u32 s[50:51], v5, v6\n"
        "v_lshlrev_b32 v2, 10, v2\n"
        "v_lshlrev_b32 v3, 10, v3\n"
        "s_waitcnt lgkmcnt(0)\n"
        "s_mov_b64 exec, vcc\n"
        "s_cbranch_execz .Ls4d_L_EF1\n"
        "global_store_dwordx2 v2, v[24:25], s[26:27]\n"
        ".Ls4d_L_EF1:\n"
        "s_mov_b64 exec, s[50:51]\n"
        "s_cbranch_execz .Ls4d_L_EF2\n"
        "global_store_dwordx2 v3, v[26:27], s[26:27]\n"
        ".Ls4d_L_EF2:\n"
        "s_endpgm\n"
        ".Ls4d_L_ERETRY:\n"
        "s_mov_b32 s3, 1048576\n"
        ".Ls4d_L_ER1:\n"
        "s_mov_b64 exec, vcc\n"
        "global_load_dwordx4 v[24:27], v122, s[28:29] offset:-4096 sc1\n"
        "global_load_dwordx4 v[28:31], v122, s[28:29] offset:0 sc1\n"
        "global_load_dwordx4 v[32:35], v123, s[28:29] offset:-4096 sc1\n"
        "global_load_dwordx4 v[36:39], v123, s[28:29] offset:0 sc1\n"
        "s_waitcnt vmcnt(0)\n"
        "v_cmp_ne_u32 vcc, s33, v25\n"
        "v_cmp_ne_u32 s[50:51], s33, v27\n"
        "v_cmp_ne_u32 s[52:53], s33, v29\n"
        "v_cmp_ne_u32 s[54:55], s33, v31\n"
        "v_cmp_ne_u32 s[56:57], s33, v33\n"
        "v_cmp_ne_u32 s[58:59], s33, v35\n"
        "v_cmp_ne_u32 s[60:61], s33, v37\n"
        "v_cmp_ne_u32 s[62:63], s33, v39\n"
        "s_or_b64 vcc, vcc, s[50:51]\n"
        "s_or_b64 s[52:53], s[52:53], s[54:55]\n"
        "s_or_b64 s[56:57], s[56:57], s[58:59]\n"
        "s_or_b64 s[60:61], s[60:61], s[62:63]\n"
        "s_or_b64 vcc, vcc, s[52:53]\n"
        "s_or_b64 s[56:57], s[56:57], s[60:61]\n"
        "s_or_b64 vcc, vcc, s[56:57]\n"
        "s_mov_b64 exec, -1\n"
        "s_cbranch_vccz .Ls4d_L_EGO\n"
        "s_sub_u32 s3, s3, 1\n"
        "s_cbranch_scc0 .Ls4d_L_ER1\n"
        "s_branch .Ls4d_L_EGO\n"
        ".Ls4d_L_M:\n"
        "v_and_b32 v239, 63, v0\n"
        "s_mul_i32 s46, s30, 7424\n"
        "s_add_u32 s46, s46, 130752\n"
        "s_lshl_b32 s47, s30, 9\n"
        "s_mov_b32 m0, s47\n"
        "v_and_b32 v44, 31, v0\n"
        "v_lshrrev_b32 v45, 5, v239\n"
        "v_mul_u32_u24 v45, 1472, v45\n"
        "v_lshl_add_u32 v44, v44, 4, v45\n"
        "s_mov_b64 vcc, 0x0fffffff\n"
        "s_mov_b32 vcc_hi, -1\n"
        "v_mov_b32 v45, -448\n"
        "v_mov_b32 v46, 1088\n"
        "v_cndmask_b32 v45, v45, v46, vcc\n"
        "v_add3_u32 v44, v44, v45, s46\n"
        "v_lshl_add_u32 v1, v239, 4, s46\n"
        "v_mov_b32 v40, 0\n"
        "v_mov_b32 v41, 0\n"
        "v_mov_b32 v42, 0\n"
        "v_mov_b32 v43, 0\n"
        "ds_write_b128 v44, v[40:43]\n"
        "ds_write_b128 v44, v[40:43] offset:2944\n"
        "ds_write_b128 v1, v[40:43] offset:6080\n"
        "v_and_b32 v6, 15, v0\n"
        "v_lshrrev_b32 v7, 4, v239\n"
        "v_lshlrev_b32 v44, 6, v7\n"
        "v_lshl_add_u32 v44, v6, 1, v44\n"
        "v_add_u32 v44, s46, v44\n"
        "v_mov_b32 v45, 0x3c00\n"
        "s_mov_b32 exec_lo, 0x07c03800\n"
        "s_mov_b32 exec_hi, 0x1ff00fe0\n"
        "ds_write_b16 v44, v45 offset:1118\n"
        "ds_write_b16 v44, v45 offset:2592\n"
        "ds_write_b16 v44, v45 offset:4066\n"
        "ds_write_b16 v44, v45 offset:5540\n"
        "s_mov_b64 exec, -1\n"
        "v_lshlrev_b32 v44, 3, v7\n"
        "v_sub_u32 v44, v44, v6\n"
        "v_lshlrev_b32 v44, 1, v44\n"
        "v_add_u32 v45, 1, v6\n"
        "v_and_b32 v45, 3, v45\n"
        "v_mul_u32_u24 v45, 1474, v45\n"
        "s_add_u32 s3, s46, 30\n"
        "v_add3_u32 v234, v44, v45, s3\n"
        "ds_read_b64 v[12:13], v234 offset:1280\n"
        "ds_read_b64 v[14:15], v234 offset:1288\n"
        "ds_read_b64 v[16:17], v234 offset:1216\n"
        "ds_read_b64 v[18:19], v234 offset:1224\n"
        "ds_read_b64 v[20:21], v234 offset:1152\n"
        "ds_read_b64 v[22:23], v234 offset:1160\n"
        "ds_read_b64 v[24:25], v234 offset:1088\n"
        "ds_read_b64 v[26:27], v234 offset:1096\n"
        "s_mov_b32 s50, 0x76543210\n"
        "s_mov_b32 s51, 0xc7654321\n"
        "s_mov_b32 s52, 0xdbaaa988\n"
        "s_mov_b32 s53, 0xddbaaa98\n"
        "v_lshlrev_b32 v45, 2, v6\n"
        "v_lshrrev_b64 v[46:47], v45, s[50:51]\n"
        "v_lshrrev_b64 v[48:49], v45, s[52:53]\n"
        "v_lshl_add_u32 v44, v7, 4, s47\n"
        "v_and_b32 v46, 15, v46\n"
        "v_and_b32 v48, 15, v48\n"
        "v_add_u32 v45, 14, v6\n"
        "s_movk_i32 s3, 2080\n"
        "v_mad_u32_u24 v248, v46, s3, v44\n"
        "v_mad_u32_u24 v249, v48, s3, v44\n"
        "v_mad_u32_u24 v250, v45, s3, v44\n"
        "s_add_u32 s12, s46, 312\n"
        "s_add_u32 s13, s46, 58\n"
        "s_mul_i32 s14, s30, 4608\n"
        "s_add_u32 s14, s14, 112320\n"
        "s_lshl_b32 s15, s30, 13\n"
        "s_add_u32 s16, s46, 6012\n"
        "s_add_u32 s17, s46, 7092\n"
        "s_add_u32 s18, s46, 5948\n"
        "s_and_b32 s45, s30, 3\n"
        "s_lshl_b32 s3, s45, 4\n"
        "s_waitcnt lgkmcnt(0)\n"
        "s_lshr_b64 s[38:39], s[38:39], s3\n"
        "s_lshl_b32 s38, s38, 16\n"
        "s_add_u32 s33, s33, 1\n"
        "v_mov_b32 v52, 0x3fb8aa3b\n"
        "v_mul_f32 v52, s38, v52\n"
        "v_exp_f32 v52, v52\n"
        "s_nop 0\n"
        "v_cvt_pk_bf16_f32 v52, v52, v52\n"
        "v_and_b32 v52, 0xffff0000, v52\n"
        "v_mul_f32 v53, 0x3e22f983, v52\n"
        "v_mul_f32 v54, 0xbfb8aa3b, v52\n"
        "s_lshl_b32 s3, s30, 2\n"
        "s_add_u32 s3, s3, 160464\n"
        "v_mov_b32 v55, s3\n"
        "s_waitcnt vmcnt(1)\n"
        "v_lshlrev_b32 v56, 16, v2\n"
        "v_and_b32 v57, 0xffff0000, v2\n"
        "v_mul_f32 v56, 0x3fb8aa3b, v56\n"
        "v_mul_f32 v57, 0x3fb8aa3b, v57\n"
        "v_and_b32 v44, 0x7fff7fff, v4\n"
        "v_and_b32 v45, 0x7fff7fff, v5\n"
        "v_exp_f32 v56, v56\n"
        "v_pk_max_u16 v44, v44, v45\n"
        "v_exp_f32 v57, v57\n"
        "v_lshlrev_b32 v45, 16, v44\n"
        "v_lshlrev_b32 v58, 16, v3\n"
        "v_max_u32 v44, v44, v45\n"
        "v_and_b32 v59, 0xffff0000, v3\n"
        "v_cvt_pk_bf16_f32 v57, v56, v57\n"
        "v_max_u32_dpp v44, v44, v44 row_ror:8 row_mask:0xf bank_mask:0xf bound_ctrl:1\n"
        "v_mul_f32 v58, v58, v53\n"
        "v_mul_f32 v59, v59, v53\n"
        "v_max_u32_dpp v44, v44, v44 row_ror:4 row_mask:0xf bank_mask:0xf bound_ctrl:1\n"
        "v_lshlrev_b32 v56, 16, v57\n"
        "v_and_b32 v57, 0xffff0000, v57\n"
        "v_max_u32_dpp v44, v44, v44 row_ror:2 row_mask:0xf bank_mask:0xf bound_ctrl:1\n"
        "v_fract_f32 v142, v58\n"
        "v_fract_f32 v144, v59\n"
        "v_max_u32_dpp v44, v44, v44 row_ror:1 row_mask:0xf bank_mask:0xf bound_ctrl:1\n"
        "v_mul_f32 v56, v56, v54\n"
        "v_mul_f32 v57, v57, v54\n"
        "v_max_u32_dpp v44, v44, v44 row_bcast:15 row_mask:0xa bank_mask:0xf\n"
        "v_cos_f32 v130, v142\n"
        "v_sin_f32 v131, v142\n"
        "v_max_u32_dpp v44, v44, v44 row_bcast:31 row_mask:0xc bank_mask:0xf\n"
        "v_exp_f32 v146, v56\n"
        "v_readlane_b32 s50, v44, 63\n"
        "v_cos_f32 v132, v144\n"
        "v_sin_f32 v133, v144\n"
        "v_exp_f32 v147, v57\n"
        "s_lshr_b32 s50, s50, 23\n"
        "s_max_u32 s50, s50, 87\n"
        "s_min_u32 s50, s50, 227\n"
        "s_sub_u32 s52, 260, s50\n"
        "s_lshl_b32 s52, s52, 23\n"
        "s_sub_u32 s50, s50, 5\n"
        "s_lshl_b32 s50, s50, 23\n"
        "v_mul_f32 v58, 0x43800000, v58\n"
        "v_mul_f32 v59, 0x43800000, v59\n"
        "v_mul_f32 v56, 0x43800000, v56\n"
        "v_mul_f32 v57, 0x43800000, v57\n"
        "v_fract_f32 v58, v58\n"
        "v_fract_f32 v59, v59\n"
        "v_exp_f32 v56, v56\n"
        "v_exp_f32 v57, v57\n"
        "v_cos_f32 v134, v58\n"
        "v_sin_f32 v135, v58\n"
        "v_cos_f32 v136, v59\n"
        "v_sin_f32 v137, v59\n"
        "v_lshlrev_b32 v138, 16, v4\n"
        "v_lshlrev_b32 v139, 16, v5\n"
        "v_and_b32 v140, 0xffff0000, v4\n"
        "v_and_b32 v141, 0xffff0000, v5\n"
        "v_mul_f32 v44, s50, v52\n"
        ".p2align 3\n"
        "v_pk_mul_f32 v[130:131], v[130:131], v[146:147] op_sel_hi:[1,0]\n"
        "v_pk_mul_f32 v[132:133], v[132:133], v[146:147] op_sel:[0,1] op_sel_hi:[1,1]\n"
        "v_pk_mul_f32 v[138:139], v[138:139], s[52:53] op_sel_hi:[1,0]\n"
        "v_pk_mul_f32 v[140:141], v[140:141], s[52:53] op_sel_hi:[1,0]\n"
        "v_pk_mul_f32 v[134:135], v[134:135], v[56:57] op_sel_hi:[1,0]\n"
        "v_pk_mul_f32 v[136:137], v[136:137], v[56:57] op_sel:[0,1] op_sel_hi:[1,1]\n"
        "s_mov_b64 exec, 1\n"
        "ds_write_b32 v55, v44\n"
        "s_mov_b64 exec, -1\n"
        "v_pk_mul_f32 v[142:143], v[134:135], v[138:139] op_sel:[1,1] op_sel_hi:[0,1] neg_lo:[1,0]\n"
        "v_pk_mul_f32 v[144:145], v[136:137], v[140:141] op_sel:[1,1] op_sel_hi:[0,1] neg_lo:[1,0]\n"
        "v_cvt_pk_f16_f32 v154, v138, -v139\n"
        "v_cvt_pk_f16_f32 v155, v140, -v141\n"
        "v_pk_fma_f32 v[146:147], v[134:135], v[138:139], v[142:143] op_sel_hi:[1,0,1]\n"
        "ds_write_addtid_b32 v154 offset:0\n"
        "v_pk_fma_f32 v[148:149], v[136:137], v[140:141], v[144:145] op_sel_hi:[1,0,1]\n"
        "ds_write_addtid_b32 v155 offset:256\n"
        "v_pk_mul_f32 v[142:143], v[130:131], v[138:139] op_sel:[1,1] op_sel_hi:[0,1] neg_lo:[1,0]\n"
        "v_pk_mul_f32 v[144:145], v[132:133], v[140:141] op_sel:[1,1] op_sel_hi:[0,1] neg_lo:[1,0]\n"
        "v_cvt_pk_f16_f32 v156, v146, -v147\n"
        "v_cvt_pk_f16_f32 v157, v148, -v149\n"
        "v_pk_fma_f32 v[138:139], v[130:131], v[138:139], v[142:143] op_sel_hi:[1,0,1]\n"
        "ds_write_addtid_b32 v156 offset:16640\n"
        "v_pk_fma_f32 v[140:141], v[132:133], v[140:141], v[144:145] op_sel_hi:[1,0,1]\n"
        "ds_write_addtid_b32 v157 offset:16896\n"
        "v_pk_mul_f32 v[142:143], v[130:131], v[138:139] op_sel:[1,1] op_sel_hi:[0,1] neg_lo:[1,0]\n"
        "v_pk_mul_f32 v[144:145], v[132:133], v[140:141] op_sel:[1,1] op_sel_hi:[0,1] neg_lo:[1,0]\n"
        "v_cvt_pk_f16_f32 v154, v138, -v139\n"
        "v_cvt_pk_f16_f32 v155, v140, -v141\n"
        "v_pk_fma_f32 v[138:139], v[130:131], v[138:139], v[142:143] op_sel_hi:[1,0,1]\n"
        "ds_write_addtid_b32 v154 offset:2080\n"
        "v_pk_fma_f32 v[140:141], v[132:133], v[140:141], v[144:145] op_sel_hi:[1,0,1]\n"
        "ds_write_addtid_b32 v155 offset:2336\n"
        "v_pk_mul_f32 v[142:143], v[134:135], v[138:139] op_sel:[1,1] op_sel_hi:[0,1] neg_lo:[1,0]\n"
        "v_pk_mul_f32 v[144:145], v[136:137], v[140:141] op_sel:[1,1] op_sel_hi:[0,1] neg_lo:[1,0]\n"
        "v_cvt_pk_f16_f32 v156, v138, -v139\n"
        "v_cvt_pk_f16_f32 v157, v140, -v141\n"
        "v_pk_fma_f32 v[146:147], v[134:135], v[138:139], v[142:143] op_sel_hi:[1,0,1]\n"
        "ds_write_addtid_b32 v156 offset:4160\n"
        "v_pk_fma_f32 v[148:149], v[136:137], v[140:141], v[144:145] op_sel_hi:[1,0,1]\n"
        "ds_write_addtid_b32 v157 offset:4416\n"
        "v_pk_mul_f32 v[142:143], v[130:131], v[138:139] op_sel:[1,1] op_sel_hi:[0,1] neg_lo:[1,0]\n"
        "v_pk_mul_f32 v[144:145], v[132:133], v[140:141] op_sel:[1,1] op_sel_hi:[0,1] neg_lo:[1,0]\n"
        "v_cvt_pk_f16_f32 v154, v146, -v147\n"
        "v_cvt_pk_f16_f32 v155, v148, -v149\n"
        "v_pk_fma_f32 v[138:139], v[130:131], v[138:139], v[142:143] op_sel_hi:[1,0,1]\n"
        "ds_write_addtid_b32 v154 offset:18720\n"
        "v_pk_fma_f32 v[140:141], v[132:133], v[140:141], v[144:145] op_sel_hi:[1,0,1]\n"
        "ds_write_addtid_b32 v155 offset:18976\n"
        "v_pk_mul_f32 v[142:143], v[130:131], v[138:139] op_sel:[1,1] op_sel_hi:[0,1] neg_lo:[1,0]\n"
        "v_pk_mul_f32 v[144:145], v[132:133], v[140:141] op_sel:[1,1] op_sel_hi:[0,1] neg_lo:[1,0]\n"
        "v_cvt_pk_f16_f32 v156, v138, -v139\n"
        "v_cvt_pk_f16_f32 v157, v140, -v141\n"
        "v_pk_fma_f32 v[138:139], v[130:131], v[138:139], v[142:143] op_sel_hi:[1,0,1]\n"
        "ds_write_addtid_b32 v156 offset:6240\n"
        "v_pk_fma_f32 v[140:141], v[132:133], v[140:141], v[144:145] op_sel_hi:[1,0,1]\n"
        "ds_write_addtid_b32 v157 offset:6496\n"
        "v_pk_mul_f32 v[142:143], v[134:135], v[138:139] op_sel:[1,1] op_sel_hi:[0,1] neg_lo:[1,0]\n"
        "v_pk_mul_f32 v[144:145], v[136:137], v[140:141] op_sel:[1,1] op_sel_hi:[0,1] neg_lo:[1,0]\n"
        "v_cvt_pk_f16_f32 v154, v138, -v139\n"
        "v_cvt_pk_f16_f32 v155, v140, -v141\n"
        "v_pk_fma_f32 v[150:151], v[134:135], v[138:139], v[142:143] op_sel_hi:[1,0,1]\n"
        "ds_write_addtid_b32 v154 offset:8320\n"
        "v_pk_fma_f32 v[152:153], v[136:137], v[140:141], v[144:145] op_sel_hi:[1,0,1]\n"
        "ds_write_addtid_b32 v155 offset:8576\n"
        "v_pk_mul_f32 v[142:143], v[130:131], v[138:139] op_sel:[1,1] op_sel_hi:[0,1] neg_lo:[1,0]\n"
        "v_pk_mul_f32 v[144:145], v[132:133], v[140:141] op_sel:[1,1] op_sel_hi:[0,1] neg_lo:[1,0]\n"
        "v_cvt_pk_f16_f32 v156, v150, -v151\n"
        "v_cvt_pk_f16_f32 v157, v152, -v153\n"
        "v_pk_fma_f32 v[138:139], v[130:131], v[138:139], v[142:143] op_sel_hi:[1,0,1]\n"
        "ds_write_addtid_b32 v156 offset:20800\n"
        "v_pk_fma_f32 v[140:141], v[132:133], v[140:141], v[144:145] op_sel_hi:[1,0,1]\n"
        "ds_write_addtid_b32 v157 offset:21056\n"
        "v_pk_mul_f32 v[142:143], v[130:131], v[138:139] op_sel:[1,1] op_sel_hi:[0,1] neg_lo:[1,0]\n"
        "v_pk_mul_f32 v[144:145], v[132:133], v[140:141] op_sel:[1,1] op_sel_hi:[0,1] neg_lo:[1,0]\n"
        "v_cvt_pk_f16_f32 v154, v138, -v139\n"
        "v_cvt_pk_f16_f32 v155, v140, -v141\n"
        "v_pk_fma_f32 v[138:139], v[130:131], v[138:139], v[142:143] op_sel_hi:[1,0,1]\n"
        "ds_write_addtid_b32 v154 offset:10400\n"
        "v_pk_fma_f32 v[140:141], v[132:133], v[140:141], v[144:145] op_sel_hi:[1,0,1]\n"
        "ds_write_addtid_b32 v155 offset:10656\n"
        "v_pk_mul_f32 v[142:143], v[134:135], v[138:139] op_sel:[1,1] op_sel_hi:[0,1] neg_lo:[1,0]\n"
        "v_pk_mul_f32 v[144:145], v[136:137], v[140:141] op_sel:[1,1] op_sel_hi:[0,1] neg_lo:[1,0]\n"
        "v_cvt_pk_f16_f32 v156, v138, -v139\n"
        "v_cvt_pk_f16_f32 v157, v140, -v141\n"
        "v_pk_fma_f32 v[146:147], v[134:135], v[138:139], v[142:143] op_sel_hi:[1,0,1]\n"
        "ds_write_addtid_b32 v156 offset:12480\n"
        "v_pk_fma_f32 v[148:149], v[136:137], v[140:141], v[144:145] op_sel_hi:[1,0,1]\n"
        "ds_write_addtid_b32 v157 offset:12736\n"
        "v_pk_mul_f32 v[142:143], v[130:131], v[138:139] op_sel:[1,1] op_sel_hi:[0,1] neg_lo:[1,0]\n"
        "v_pk_mul_f32 v[144:145], v[132:133], v[140:141] op_sel:[1,1] op_sel_hi:[0,1] neg_lo:[1,0]\n"
        "v_cvt_pk_f16_f32 v154, v146, -v147\n"
        "v_cvt_pk_f16_f32 v155, v148, -v149\n"
        "v_pk_fma_f32 v[138:139], v[130:131], v[138:139], v[142:143] op_sel_hi:[1,0,1]\n"
        "ds_write_addtid_b32 v154 offset:22880\n"
        "v_pk_fma_f32 v[140:141], v[132:133], v[140:141], v[144:145] op_sel_hi:[1,0,1]\n"
        "ds_write_addtid_b32 v155 offset:23136\n"
        "v_pk_mul_f32 v[142:143], v[130:131], v[138:139] op_sel:[1,1] op_sel_hi:[0,1] neg_lo:[1,0]\n"
        "v_pk_mul_f32 v[144:145], v[132:133], v[140:141] op_sel:[1,1] op_sel_hi:[0,1] neg_lo:[1,0]\n"
        "v_cvt_pk_f16_f32 v156, v138, -v139\n"
        "v_cvt_pk_f16_f32 v157, v140, -v141\n"
        "v_pk_fma_f32 v[138:139], v[130:131], v[138:139], v[142:143] op_sel_hi:[1,0,1]\n"
        "ds_write_addtid_b32 v156 offset:14560\n"
        "v_pk_fma_f32 v[140:141], v[132:133], v[140:141], v[144:145] op_sel_hi:[1,0,1]\n"
        "ds_write_addtid_b32 v157 offset:14816\n"
        "v_pk_mul_f32 v[142:143], v[134:135], v[138:139] op_sel:[1,1] op_sel_hi:[0,1] neg_lo:[1,0]\n"
        "v_pk_mul_f32 v[144:145], v[136:137], v[140:141] op_sel:[1,1] op_sel_hi:[0,1] neg_lo:[1,0]\n"
        "v_cvt_pk_f16_f32 v154, v138, -v139\n"
        "v_cvt_pk_f16_f32 v155, v140, -v141\n"
        "v_pk_fma_f32 v[146:147], v[134:135], v[138:139], v[142:143] op_sel_hi:[1,0,1]\n"
        "ds_write_addtid_b32 v154 offset:24960\n"
        "v_pk_fma_f32 v[148:149], v[136:137], v[140:141], v[144:145] op_sel_hi:[1,0,1]\n"
        "ds_write_addtid_b32 v155 offset:25216\n"
        "v_pk_mul_f32 v[142:143], v[134:135], v[150:151] op_sel:[1,1] op_sel_hi:[0,1] neg_lo:[1,0]\n"
        "v_pk_mul_f32 v[144:145], v[136:137], v[152:153] op_sel:[1,1] op_sel_hi:[0,1] neg_lo:[1,0]\n"
        "v_cvt_pk_f16_f32 v156, v146, -v147\n"
        "v_cvt_pk_f16_f32 v157, v148, -v149\n"
        "v_pk_fma_f32 v[150:151], v[134:135], v[150:151], v[142:143] op_sel_hi:[1,0,1]\n"
        "ds_write_addtid_b32 v156 offset:27040\n"
        "v_pk_fma_f32 v[152:153], v[136:137], v[152:153], v[144:145] op_sel_hi:[1,0,1]\n"
        "ds_write_addtid_b32 v157 offset:27296\n"
        "v_pk_mul_f32 v[142:143], v[134:135], v[146:147] op_sel:[1,1] op_sel_hi:[0,1] neg_lo:[1,0]\n"
        "ds_read_b128 v[28:31], v248 offset:0\n"
        "v_pk_mul_f32 v[144:145], v[136:137], v[148:149] op_sel:[1,1] op_sel_hi:[0,1] neg_lo:[1,0]\n"
        "v_cvt_pk_f16_f32 v154, v150, -v151\n"
        "v_cvt_pk_f16_f32 v155, v152, -v153\n"
        "ds_read_b128 v[32:35], v248 offset:64\n"
        "v_pk_fma_f32 v[146:147], v[134:135], v[146:147], v[142:143] op_sel_hi:[1,0,1]\n"
        "ds_write_addtid_b32 v154 offset:29120\n"
        "v_pk_fma_f32 v[148:149], v[136:137], v[148:149], v[144:145] op_sel_hi:[1,0,1]\n"
        "ds_write_addtid_b32 v155 offset:29376\n"
        "v_pk_mul_f32 v[142:143], v[134:135], v[150:151] op_sel:[1,1] op_sel_hi:[0,1] neg_lo:[1,0]\n"
        "ds_read_b128 v[36:39], v248 offset:128\n"
        "v_pk_mul_f32 v[144:145], v[136:137], v[152:153] op_sel:[1,1] op_sel_hi:[0,1] neg_lo:[1,0]\n"
        "v_cvt_pk_f16_f32 v156, v146, -v147\n"
        "v_cvt_pk_f16_f32 v157, v148, -v149\n"
        "ds_read_b128 v[40:43], v248 offset:192\n"
        "v_pk_fma_f32 v[150:151], v[134:135], v[150:151], v[142:143] op_sel_hi:[1,0,1]\n"
        "ds_write_addtid_b32 v156 offset:31200\n"
        "v_pk_fma_f32 v[152:153], v[136:137], v[152:153], v[144:145] op_sel_hi:[1,0,1]\n"
        "ds_write_addtid_b32 v157 offset:31456\n"
        "v_pk_mul_f32 v[142:143], v[134:135], v[146:147] op_sel:[1,1] op_sel_hi:[0,1] neg_lo:[1,0]\n"
        "ds_read_b128 v[44:47], v248 offset:256\n"
        "v_pk_mul_f32 v[144:145], v[136:137], v[148:149] op_sel:[1,1] op_sel_hi:[0,1] neg_lo:[1,0]\n"
        "v_cvt_pk_f16_f32 v154, v150, -v151\n"
        "v_cvt_pk_f16_f32 v155, v152, -v153\n"
        "ds_read_b128 v[48:51], v248 offset:320\n"
        "v_pk_fma_f32 v[146:147], v[134:135], v[146:147], v[142:143] op_sel_hi:[1,0,1]\n"
        "ds_write_addtid_b32 v154 offset:33280\n"
        "v_pk_fma_f32 v[148:149], v[136:137], v[148:149], v[144:145] op_sel_hi:[1,0,1]\n"
        "ds_write_addtid_b32 v155 offset:33536\n"
        "v_pk_mul_f32 v[142:143], v[134:135], v[146:147] op_sel:[1,1] op_sel_hi:[0,1] neg_lo:[1,0]\n"
        "ds_read_b128 v[52:55], v248 offset:384\n"
        "v_pk_mul_f32 v[144:145], v[136:137], v[148:149] op_sel:[1,1] op_sel_hi:[0,1] neg_lo:[1,0]\n"
        "v_cvt_pk_f16_f32 v156, v146, -v147\n"
        "v_cvt_pk_f16_f32 v157, v148, -v149\n"
        "ds_read_b128 v[56:59], v248 offset:448\n"
        "v_pk_fma_f32 v[146:147], v[134:135], v[146:147], v[142:143] op_sel_hi:[1,0,1]\n"
        "ds_write_addtid_b32 v156 offset:35360\n"
        "v_pk_fma_f32 v[148:149], v[136:137], v[148:149], v[144:145] op_sel_hi:[1,0,1]\n"
        "ds_write_addtid_b32 v157 offset:35616\n"
        "v_pk_mul_f32 v[142:143], v[134:135], v[146:147] op_sel:[1,1] op_sel_hi:[0,1] neg_lo:[1,0]\n"
        "ds_read_b128 v[60:63], v249 offset:0\n"
        "v_pk_mul_f32 v[144:145], v[136:137], v[148:149] op_sel:[1,1] op_sel_hi:[0,1] neg_lo:[1,0]\n"
        "v_cvt_pk_f16_f32 v154, v146, -v147\n"
        "v_cvt_pk_f16_f32 v155, v148, -v149\n"
        "ds_read_b128 v[64:67], v249 offset:64\n"
        "v_pk_fma_f32 v[146:147], v[134:135], v[146:147], v[142:143] op_sel_hi:[1,0,1]\n"
        "ds_write_addtid_b32 v154 offset:37440\n"
        "v_pk_fma_f32 v[148:149], v[136:137], v[148:149], v[144:145] op_sel_hi:[1,0,1]\n"
        "ds_write_addtid_b32 v155 offset:37696\n"
        "v_pk_mul_f32 v[142:143], v[134:135], v[146:147] op_sel:[1,1] op_sel_hi:[0,1] neg_lo:[1,0]\n"
        "ds_read_b128 v[68:71], v249 offset:128\n"
        "v_pk_mul_f32 v[144:145], v[136:137], v[148:149] op_sel:[1,1] op_sel_hi:[0,1] neg_lo:[1,0]\n"
        "v_cvt_pk_f16_f32 v156, v146, -v147\n"
        "v_cvt_pk_f16_f32 v157, v148, -v149\n"
        "ds_read_b128 v[72:75], v249 offset:192\n"
        "v_pk_fma_f32 v[146:147], v[134:135], v[146:147], v[142:143] op_sel_hi:[1,0,1]\n"
        "ds_write_addtid_b32 v156 offset:39520\n"
        "v_pk_fma_f32 v[148:149], v[136:137], v[148:149], v[144:145] op_sel_hi:[1,0,1]\n"
        "ds_write_addtid_b32 v157 offset:39776\n"
        "v_pk_mul_f32 v[142:143], v[134:135], v[146:147] op_sel:[1,1] op_sel_hi:[0,1] neg_lo:[1,0]\n"
        "ds_read_b128 v[76:79], v249 offset:256\n"
        "v_pk_mul_f32 v[144:145], v[136:137], v[148:149] op_sel:[1,1] op_sel_hi:[0,1] neg_lo:[1,0]\n"
        "v_cvt_pk_f16_f32 v154, v146, -v147\n"
        "v_cvt_pk_f16_f32 v155, v148, -v149\n"
        "ds_read_b128 v[80:83], v249 offset:320\n"
        "v_pk_fma_f32 v[146:147], v[134:135], v[146:147], v[142:143] op_sel_hi:[1,0,1]\n"
        "ds_write_addtid_b32 v154 offset:41600\n"
        "v_pk_fma_f32 v[148:149], v[136:137], v[148:149], v[144:145] op_sel_hi:[1,0,1]\n"
        "ds_write_addtid_b32 v155 offset:41856\n"
        "v_cvt_pk_f16_f32 v156, v146, -v147\n"
        "v_cvt_pk_f16_f32 v157, v148, -v149\n"
        "ds_write_addtid_b32 v156 offset:43680\n"
        "ds_write_addtid_b32 v157 offset:43936\n"
        "ds_read_b128 v[84:87], v249 offset:384\n"
        "ds_read_b128 v[88:91], v249 offset:448\n"
        "ds_read_b128 v[96:99], v250 offset:0\n"
        "ds_read_b128 v[100:103], v250 offset:64\n"
        "ds_read_b128 v[104:107], v250 offset:128\n"
        "ds_read_b128 v[108:111], v250 offset:192\n"
        "ds_read_b128 v[112:115], v250 offset:256\n"
        "ds_read_b128 v[116:119], v250 offset:320\n"
        "ds_read_b128 v[120:123], v250 offset:384\n"
        "ds_read_b128 v[124:127], v250 offset:448\n"
        "s_lshl_b32 s3, s34, 7\n"
        "s_add_u32 s3, s3, 266240\n"
        "v_mov_b32 v2, s3\n"
        "v_mov_b32 v4, 0\n"
        "v_mov_b32 v5, s33\n"
        "s_lshl_b32 s3, s34, 5\n"
        "s_add_u32 s3, s3, 282624\n"
        "s_add_u32 s50, s28, s3\n"
        "s_addc_u32 s51, s29, 0\n"
        "s_add_u32 s52, s50, 0x10000\n"
        "s_addc_u32 s53, s51, 0\n"
        "s_add_u32 s54, s52, 0x10000\n"
        "s_addc_u32 s55, s53, 0\n"
        "s_add_u32 s56, s54, 0x10000\n"
        "s_addc_u32 s57, s55, 0\n"
        "s_add_u32 s58, s56, 0x10000\n"
        "s_addc_u32 s59, s57, 0\n"
        "s_cmp_eq_u32 s30, 3\n"
        "s_waitcnt lgkmcnt(0)\n"
        "s_barrier\n"
        ".Ls4d_L_MS:\n"
        "ds_read_b128 v[128:131], v250 offset:16640\n"
        "ds_read_b128 v[160:163], v250 offset:49920\n"
        "ds_read_b128 v[132:135], v250 offset:16704\n"
        "ds_read_b128 v[164:167], v250 offset:49984\n"
        "ds_read_b128 v[136:139], v250 offset:16768\n"
        "s_cbranch_scc0 .Ls4d_L_M2\n"
        "s_mov_b64 exec, 1\n"
        "global_store_dwordx2 v2, v[4:5], s[28:29] sc1\n"
        "s_mov_b64 exec, -1\n"
        ".Ls4d_L_M2:\n"
        "s_waitcnt lgkmcnt(4)\n"
        "v_mfma_f32_16x16x32_f16 v[192:195], v[28:31], v[128:131], 0\n"
        "ds_read_b128 v[168:171], v250 offset:50048\n"
        "v_and_b32 v224, 1, v7\n"
        "v_lshrrev_b32 v225, 1, v7\n"
        "s_waitcnt lgkmcnt(4)\n"
        "v_mfma_f32_16x16x32_f16 v[196:199], v[28:31], v[160:163], 0\n"
        "ds_read_b128 v[140:143], v250 offset:16832\n"
        "v_lshlrev_b32 v224, 3, v224\n"
        "v_mul_u32_u24 v226, 1472, v225\n"
        "s_waitcnt lgkmcnt(4)\n"
        "v_mfma_f32_16x16x32_f16 v[192:195], v[32:35], v[132:135], v[192:195]\n"
        "ds_read_b128 v[172:175], v250 offset:50112\n"
        "v_lshl_add_u32 v224, v6, 4, v224\n"
        "v_lshl_add_u32 v225, v225, 4, v6\n"
        "s_waitcnt lgkmcnt(4)\n"
        "v_mfma_f32_16x16x32_f16 v[196:199], v[32:35], v[164:167], v[196:199]\n"
        "ds_read_b128 v[144:147], v250 offset:16896\n"
        "v_sub_u32 v224, v226, v224\n"
        "v_lshl_add_u32 v246, v225, 1, s46\n"
        "s_waitcnt lgkmcnt(4)\n"
        "v_mfma_f32_16x16x32_f16 v[192:195], v[36:39], v[136:139], v[192:195]\n"
        "ds_read_b128 v[176:179], v250 offset:50176\n"
        "v_add_u32 v243, s12, v224\n"
        "v_lshrrev_b32 v225, 1, v225\n"
        "s_waitcnt lgkmcnt(4)\n"
        "v_mfma_f32_16x16x32_f16 v[196:199], v[36:39], v[168:171], v[196:199]\n"
        "ds_read_b128 v[148:151], v250 offset:16960\n"
        "v_add_u32 v244, 0xb84, v243\n"
        "v_and_b32 v224, 1, v6\n"
        "s_waitcnt lgkmcnt(4)\n"
        "v_mfma_f32_16x16x32_f16 v[192:195], v[40:43], v[140:143], v[192:195]\n"
        "ds_read_b128 v[180:183], v250 offset:50240\n"
        "v_lshlrev_b32 v225, 2, v225\n"
        "v_lshl_add_u32 v226, v224, 8, s16\n"
        "s_waitcnt lgkmcnt(4)\n"
        "v_mfma_f32_16x16x32_f16 v[196:199], v[40:43], v[172:175], v[196:199]\n"
        "ds_read_b128 v[152:155], v250 offset:17024\n"
        "v_sub_u32 v245, v226, v225\n"
        "v_xor_b32 v224, 1, v224\n"
        "s_waitcnt lgkmcnt(4)\n"
        "v_mfma_f32_16x16x32_f16 v[192:195], v[44:47], v[144:147], v[192:195]\n"
        "ds_read_b128 v[184:187], v250 offset:50304\n"
        "v_mul_u32_u24 v224, 644, v224\n"
        "v_lshlrev_b32 v225, 2, v7\n"
        "s_waitcnt lgkmcnt(4)\n"
        "v_mfma_f32_16x16x32_f16 v[196:199], v[44:47], v[176:179], v[196:199]\n"
        "ds_read_b128 v[156:159], v250 offset:17088\n"
        "v_sub_u32 v225, v6, v225\n"
        "v_add_u32 v224, s18, v224\n"
        "s_waitcnt lgkmcnt(4)\n"
        "v_mfma_f32_16x16x32_f16 v[192:195], v[48:51], v[148:151], v[192:195]\n"
        "ds_read_b128 v[188:191], v250 offset:50368\n"
        "v_lshlrev_b32 v225, 2, v225\n"
        "v_and_b32 v226, 3, v0\n"
        "s_waitcnt lgkmcnt(4)\n"
        "v_mfma_f32_16x16x32_f16 v[196:199], v[48:51], v[180:183], v[196:199]\n"
        "v_sub_u32 v237, v224, v225\n"
        "v_add_u32 v238, s17, v225\n"
        "s_waitcnt lgkmcnt(3)\n"
        "v_mfma_f32_16x16x32_f16 v[192:195], v[52:55], v[152:155], v[192:195]\n"
        "v_mul_u32_u24 v224, 1474, v226\n"
        "v_lshrrev_b32 v225, 2, v239\n"
        "s_waitcnt lgkmcnt(2)\n"
        "v_mfma_f32_16x16x32_f16 v[196:199], v[52:55], v[184:187], v[196:199]\n"
        "v_lshlrev_b32 v226, 3, v226\n"
        "v_lshl_add_u32 v224, v225, 1, v224\n"
        "s_waitcnt lgkmcnt(1)\n"
        "v_mfma_f32_16x16x32_f16 v[192:195], v[56:59], v[156:159], v[192:195]\n"
        "v_lshl_add_u32 v240, v225, 12, v226\n"
        "v_add_u32 v247, s13, v224\n"
        "s_waitcnt lgkmcnt(0)\n"
        "v_mfma_f32_16x16x32_f16 v[196:199], v[56:59], v[188:191], v[196:199]\n"
        "v_lshl_add_u32 v224, v7, 4, s14\n"
        "v_lshl_add_u32 v235, v6, 5, v224\n"
        "v_mfma_f32_16x16x32_f16 v[200:203], v[60:63], v[128:131], 0\n"
        "s_mul_i32 m0, s30, 8704\n"
        "v_mfma_f32_16x16x32_f16 v[204:207], v[60:63], v[160:163], 0\n"
        "v_mfma_f32_16x16x32_f16 v[200:203], v[64:67], v[132:135], v[200:203]\n"
        "v_mfma_f32_16x16x32_f16 v[204:207], v[64:67], v[164:167], v[204:207]\n"
        "v_cvt_pk_f16_f32 v216, v195, v194\n"
        "v_cvt_pk_f16_f32 v217, v193, v192\n"
        "v_mfma_f32_16x16x32_f16 v[200:203], v[68:71], v[136:139], v[200:203]\n"
        "v_cvt_pk_f16_f32 v218, v199, v198\n"
        "v_cvt_pk_f16_f32 v219, v197, v196\n"
        "v_mfma_f32_16x16x32_f16 v[204:207], v[68:71], v[168:171], v[204:207]\n"
        "ds_write_b64 v243, v[216:217] offset:768\n"
        "ds_write2_b32 v244, v216, v217 offset0:192 offset1:193\n"
        "v_mfma_f32_16x16x32_f16 v[200:203], v[72:75], v[140:143], v[200:203]\n"
        "ds_write_b64 v243, v[218:219] offset:512\n"
        "ds_write2_b32 v244, v218, v219 offset0:128 offset1:129\n"
        "v_mfma_f32_16x16x32_f16 v[204:207], v[72:75], v[172:175], v[204:207]\n"
        "v_readlane_b32 s60, v192, 0\n"
        "v_mfma_f32_16x16x32_f16 v[200:203], v[76:79], v[144:147], v[200:203]\n"
        "s_nop 0\n"
        "v_cvt_pk_f16_f32 v220, s60, s60\n"
        "v_mfma_f32_16x16x32_f16 v[204:207], v[76:79], v[176:179], v[204:207]\n"
        "s_mov_b64 exec, 0xf\n"
        "ds_write_b16 v247, v220 offset:1028\n"
        "s_mov_b64 exec, -1\n"
        "ds_read_b64 v[56:57], v234 offset:576\n"
        "v_mfma_f32_16x16x32_f16 v[200:203], v[80:83], v[148:151], v[200:203]\n"
        "ds_read_b64 v[58:59], v234 offset:584\n"
        "ds_read_b64 v[52:53], v234 offset:640\n"
        "v_mfma_f32_16x16x32_f16 v[204:207], v[80:83], v[180:183], v[204:207]\n"
        "ds_read_b64 v[54:55], v234 offset:648\n"
        "ds_read_b64 v[48:49], v234 offset:704\n"
        "v_mfma_f32_16x16x32_f16 v[200:203], v[84:87], v[152:155], v[200:203]\n"
        "ds_read_b64 v[50:51], v234 offset:712\n"
        "ds_read_b64 v[44:45], v234 offset:768\n"
        "v_mfma_f32_16x16x32_f16 v[204:207], v[84:87], v[184:187], v[204:207]\n"
        "ds_read_b64 v[46:47], v234 offset:776\n"
        "ds_read_b64 v[40:41], v234 offset:832\n"
        "v_mfma_f32_16x16x32_f16 v[200:203], v[88:91], v[156:159], v[200:203]\n"
        "ds_read_b64 v[42:43], v234 offset:840\n"
        "ds_read_b64 v[36:37], v234 offset:896\n"
        "v_mfma_f32_16x16x32_f16 v[204:207], v[88:91], v[188:191], v[204:207]\n"
        "ds_read_b64 v[38:39], v234 offset:904\n"
        "ds_read_b64 v[32:33], v234 offset:960\n"
        "ds_read_b64 v[34:35], v234 offset:968\n"
        "ds_read_b64 v[28:29], v234 offset:1024\n"
        "ds_read_b64 v[30:31], v234 offset:1032\n"
        "s_barrier\n"
        "v_mfma_f32_16x16x32_f16 v[208:211], v[96:99], v[128:131], 0\n"
        "v_cvt_pk_f16_f32 v216, v203, v202\n"
        "v_cvt_pk_f16_f32 v217, v201, v200\n"
        "v_mfma_f32_16x16x32_f16 v[212:215], v[96:99], v[160:163], 0\n"
        "ds_read_b128 v[128:131], v235 offset:32\n"
        "v_mfma_f32_16x16x32_f16 v[208:211], v[100:103], v[132:135], v[208:211]\n"
        "v_cvt_pk_f16_f32 v218, v207, v206\n"
        "v_cvt_pk_f16_f32 v219, v205, v204\n"
        "v_mfma_f32_16x16x32_f16 v[212:215], v[100:103], v[164:167], v[212:215]\n"
        "ds_read_b128 v[132:135], v235 offset:96\n"
        "v_mfma_f32_16x16x32_f16 v[208:211], v[104:107], v[136:139], v[208:211]\n"
        "ds_write_b64 v243, v[216:217] offset:256\n"
        "ds_write2_b32 v244, v216, v217 offset0:64 offset1:65\n"
        "v_mfma_f32_16x16x32_f16 v[212:215], v[104:107], v[168:171], v[212:215]\n"
        "ds_read_b128 v[136:139], v235 offset:160\n"
        "v_mfma_f32_16x16x32_f16 v[208:211], v[108:111], v[140:143], v[208:211]\n"
        "ds_write_b64 v243, v[218:219] offset:0\n"
        "ds_write2_b32 v244, v218, v219 offset0:0 offset1:1\n"
        "v_mfma_f32_16x16x32_f16 v[212:215], v[108:111], v[172:175], v[212:215]\n"
        "ds_read_b128 v[140:143], v235 offset:224\n"
        "v_mfma_f32_16x16x32_f16 v[208:211], v[112:115], v[144:147], v[208:211]\n"
        "v_readlane_b32 s60, v207, 63\n"
        "v_mfma_f32_16x16x32_f16 v[212:215], v[112:115], v[176:179], v[212:215]\n"
        "ds_read_b128 v[144:147], v235 offset:288\n"
        "v_mfma_f32_16x16x32_f16 v[208:211], v[116:119], v[148:151], v[208:211]\n"
        "v_cvt_pk_f16_f32 v220, s60, s60\n"
        "v_mfma_f32_16x16x32_f16 v[212:215], v[116:119], v[180:183], v[212:215]\n"
        "ds_read_b128 v[148:151], v235 offset:352\n"
        "v_mfma_f32_16x16x32_f16 v[208:211], v[120:123], v[152:155], v[208:211]\n"
        "s_mov_b64 exec, 0xfff\n"
        "ds_write_b16 v247, v220 offset:0\n"
        "s_mov_b64 exec, -1\n"
        "ds_read_b64 v[88:89], v234 offset:64\n"
        "v_mfma_f32_16x16x32_f16 v[212:215], v[120:123], v[184:187], v[212:215]\n"
        "ds_read_b128 v[152:155], v235 offset:416\n"
        "v_mfma_f32_16x16x32_f16 v[208:211], v[124:127], v[156:159], v[208:211]\n"
        "ds_read_b64 v[90:91], v234 offset:72\n"
        "ds_read_b64 v[84:85], v234 offset:128\n"
        "v_mfma_f32_16x16x32_f16 v[212:215], v[124:127], v[188:191], v[212:215]\n"
        "ds_read_b128 v[156:159], v235 offset:480\n"
        "s_waitcnt lgkmcnt(15)\n"
        "v_mfma_f32_16x16x32_f16 v[192:195], v[56:59], v[128:131], 0\n"
        "ds_read_b64 v[86:87], v234 offset:136\n"
        "ds_read_b64 v[80:81], v234 offset:192\n"
        "s_waitcnt lgkmcnt(15)\n"
        "v_mfma_f32_16x16x32_f16 v[192:195], v[52:55], v[132:135], v[192:195]\n"
        "ds_read_b64 v[82:83], v234 offset:200\n"
        "ds_read_b64 v[76:77], v234 offset:256\n"
        "s_waitcnt lgkmcnt(15)\n"
        "v_mfma_f32_16x16x32_f16 v[192:195], v[48:51], v[136:139], v[192:195]\n"
        "ds_read_b64 v[78:79], v234 offset:264\n"
        "ds_read_b64 v[72:73], v234 offset:320\n"
        "s_waitcnt lgkmcnt(14)\n"
        "v_mfma_f32_16x16x32_f16 v[192:195], v[44:47], v[140:143], v[192:195]\n"
        "ds_read_b64 v[74:75], v234 offset:328\n"
        "ds_read_b64 v[68:69], v234 offset:384\n"
        "s_waitcnt lgkmcnt(15)\n"
        "v_mfma_f32_16x16x32_f16 v[192:195], v[40:43], v[144:147], v[192:195]\n"
        "ds_read_b64 v[70:71], v234 offset:392\n"
        "ds_read_b64 v[64:65], v234 offset:448\n"
        "s_waitcnt lgkmcnt(15)\n"
        "v_mfma_f32_16x16x32_f16 v[192:195], v[36:39], v[148:151], v[192:195]\n"
        "ds_read_b64 v[66:67], v234 offset:456\n"
        "ds_read_b64 v[60:61], v234 offset:512\n"
        "s_waitcnt lgkmcnt(15)\n"
        "v_mfma_f32_16x16x32_f16 v[192:195], v[32:35], v[152:155], v[192:195]\n"
        "ds_read_b64 v[62:63], v234 offset:520\n"
        "ds_read_b64 v[92:93], v234 offset:0\n"
        "s_waitcnt lgkmcnt(14)\n"
        "v_mfma_f32_16x16x32_f16 v[192:195], v[28:31], v[156:159], v[192:195]\n"
        "ds_read_b64 v[94:95], v234 offset:8\n"
        "v_mfma_f32_16x16x32_f16 v[196:199], v[88:91], v[128:131], 0\n"
        "v_permlane32_swap_b32 v208, v212\n"
        "v_permlane32_swap_b32 v209, v213\n"
        "s_waitcnt lgkmcnt(14)\n"
        "v_mfma_f32_16x16x32_f16 v[196:199], v[84:87], v[132:135], v[196:199]\n"
        "v_permlane32_swap_b32 v210, v214\n"
        "v_permlane32_swap_b32 v211, v215\n"
        "s_waitcnt lgkmcnt(12)\n"
        "v_mfma_f32_16x16x32_f16 v[196:199], v[80:83], v[136:139], v[196:199]\n"
        "v_cvt_pk_f16_f32 v216, v208, v209\n"
        "v_cvt_pk_f16_f32 v217, v210, v211\n"
        "ds_write_addtid_b32 v192 offset:0\n"
        "ds_write_addtid_b32 v193 offset:272\n"
        "s_waitcnt lgkmcnt(12)\n"
        "v_mfma_f32_16x16x32_f16 v[196:199], v[76:79], v[140:143], v[196:199]\n"
        "v_readlane_b32 s60, v211, 47\n"
        "ds_write_addtid_b32 v194 offset:544\n"
        "ds_write_addtid_b32 v195 offset:816\n"
        "s_waitcnt lgkmcnt(12)\n"
        "v_mfma_f32_16x16x32_f16 v[196:199], v[72:75], v[144:147], v[196:199]\n"
        "v_cvt_pk_f16_f32 v220, s60, s60\n"
        "s_waitcnt lgkmcnt(10)\n"
        "v_mfma_f32_16x16x32_f16 v[196:199], v[68:71], v[148:151], v[196:199]\n"
        "s_mov_b64 exec, 0xff\n"
        "ds_write_b16 v247, v220 offset:1340\n"
        "s_mov_b64 exec, -1\n"
        "ds_read_b64 v[8:9], v234 offset:1344\n"
        "ds_read_b128 v[124:127], v235 offset:464\n"
        "s_waitcnt lgkmcnt(11)\n"
        "v_mfma_f32_16x16x32_f16 v[196:199], v[64:67], v[152:155], v[196:199]\n"
        "ds_read_b64 v[10:11], v234 offset:1352\n"
        "s_mov_b32 exec_lo, 0x0000ffff\n"
        "s_mov_b32 exec_hi, 0x0000ffff\n"
        "ds_write2_b32 v245, v216, v216 offset0:16 offset1:177\n"
        "ds_write2_b32 v245, v217, v217 offset0:0 offset1:161\n"
        "s_mov_b64 exec, -1\n"
        "s_waitcnt lgkmcnt(6)\n"
        "s_barrier\n"
        "v_mfma_f32_16x16x32_f16 v[196:199], v[60:63], v[156:159], v[196:199]\n"
        "s_mov_b32 exec_lo, 0xffff0000\n"
        "s_mov_b32 exec_hi, 0xffff0000\n"
        "ds_write_b16 v246, v216 offset:7104\n"
        "ds_write_b16_d16_hi v246, v216 offset:7168\n"
        "s_mov_b64 exec, -1\n"
        "v_mfma_f32_16x16x32_f16 v[200:203], v[92:95], v[156:159], 0\n"
        "s_mov_b32 exec_lo, 0xffff0000\n"
        "s_mov_b32 exec_hi, 0xffff0000\n"
        "ds_write_b16 v246, v217 offset:7232\n"
        "ds_write_b16_d16_hi v246, v217 offset:7296\n"
        "s_mov_b64 exec, -1\n"
        "v_mfma_f32_16x16x32_f16 v[128:131], v[156:159], v[24:27], 0\n"
        "ds_read_b128 v[168:171], v235 offset:544\n"
        "v_mfma_f32_16x16x32_f16 v[208:211], v[156:159], v[20:23], 0\n"
        "v_mfma_f32_16x16x32_f16 v[152:155], v[156:159], v[16:19], 0\n"
        "ds_read_b128 v[172:175], v235 offset:608\n"
        "s_waitcnt lgkmcnt(9)\n"
        "v_mfma_f32_16x16x32_f16 v[132:135], v[124:127], v[24:27], 0\n"
        "v_mfma_f32_16x16x32_f16 v[212:215], v[124:127], v[20:23], 0\n"
        "ds_read_b128 v[176:179], v235 offset:672\n"
        "v_mfma_f32_16x16x32_f16 v[216:219], v[124:127], v[12:15], 0\n"
        "s_waitcnt lgkmcnt(9)\n"
        "v_mfma_f32_16x16x32_f16 v[192:195], v[8:11], v[156:159], 0\n"
        "ds_read_b128 v[180:183], v235 offset:736\n"
        "s_waitcnt lgkmcnt(3)\n"
        "v_mfma_f32_16x16x32_f16 v[196:199], v[56:59], v[168:171], v[196:199]\n"
        "v_mfma_f32_16x16x32_f16 v[200:203], v[88:91], v[168:171], v[200:203]\n"
        "ds_read_b128 v[184:187], v235 offset:800\n"
        "v_cvt_pk_f16_f32 v128, v128, v208\n"
        "v_cvt_pk_f16_f32 v129, v129, v209\n"
        "s_waitcnt lgkmcnt(3)\n"
        "v_mfma_f32_16x16x32_f16 v[196:199], v[52:55], v[172:175], v[196:199]\n"
        "v_mfma_f32_16x16x32_f16 v[200:203], v[84:87], v[172:175], v[200:203]\n"
        "ds_read_b128 v[188:191], v235 offset:864\n"
        "v_cvt_pk_f16_f32 v130, v130, v210\n"
        "v_cvt_pk_f16_f32 v131, v131, v211\n"
        "s_waitcnt lgkmcnt(3)\n"
        "v_mfma_f32_16x16x32_f16 v[196:199], v[48:51], v[176:179], v[196:199]\n"
        "v_mfma_f32_16x16x32_f16 v[200:203], v[80:83], v[176:179], v[200:203]\n"
        "ds_read_b128 v[168:171], v235 offset:928\n"
        "v_cvt_pk_f16_f32 v132, v132, v212\n"
        "v_cvt_pk_f16_f32 v133, v133, v213\n"
        "s_waitcnt lgkmcnt(3)\n"
        "v_mfma_f32_16x16x32_f16 v[196:199], v[44:47], v[180:183], v[196:199]\n"
        "v_mfma_f32_16x16x32_f16 v[200:203], v[76:79], v[180:183], v[200:203]\n"
        "ds_read_b128 v[172:175], v235 offset:992\n"
        "v_cvt_pk_f16_f32 v134, v134, v214\n"
        "v_cvt_pk_f16_f32 v135, v135, v215\n"
        "s_waitcnt lgkmcnt(3)\n"
        "v_mfma_f32_16x16x32_f16 v[196:199], v[40:43], v[184:187], v[196:199]\n"
        "v_mfma_f32_16x16x32_f16 v[200:203], v[72:75], v[184:187], v[200:203]\n"
        "v_cvt_pk_f16_f32 v152, v152, v216\n"
        "v_cvt_pk_f16_f32 v153, v153, v217\n"
        "s_waitcnt lgkmcnt(2)\n"
        "v_mfma_f32_16x16x32_f16 v[196:199], v[36:39], v[188:191], v[196:199]\n"
        "v_mfma_f32_16x16x32_f16 v[200:203], v[68:71], v[188:191], v[200:203]\n"
        "v_cvt_pk_f16_f32 v154, v154, v218\n"
        "v_cvt_pk_f16_f32 v155, v155, v219\n"
        "s_waitcnt lgkmcnt(1)\n"
        "v_mfma_f32_16x16x32_f16 v[196:199], v[32:35], v[168:171], v[196:199]\n"
        "v_mfma_f32_16x16x32_f16 v[200:203], v[64:67], v[168:171], v[200:203]\n"
        "ds_read_b128 v[124:127], v235 offset:976\n"
        "v_and_b32 v224, 0x7fff, v240\n"
        "v_and_b32 v225, 4, v0\n"
        "s_waitcnt lgkmcnt(1)\n"
        "v_mfma_f32_16x16x32_f16 v[196:199], v[28:31], v[172:175], v[196:199]\n"
        "v_mfma_f32_16x16x32_f16 v[200:203], v[60:63], v[172:175], v[200:203]\n"
        "ds_read_b64 v[96:97], v237 offset:128\n"
        "v_add_u32 v241, 0x50000, v224\n"
        "v_lshlrev_b32 v225, 1, v225\n"
        "v_mfma_f32_16x16x32_f16 v[204:207], v[92:95], v[172:175], 0\n"
        "ds_read_b64 v[98:99], v237 offset:136\n"
        "v_mfma_f32_16x16x32_f16 v[136:139], v[172:175], v[24:27], 0\n"
        "ds_read_b128 v[176:179], v235 offset:1056\n"
        "v_add_u32 v242, 0x9d000, v225\n"
        "s_nop 0\n"
        "v_mfma_f32_16x16x32_f16 v[208:211], v[172:175], v[20:23], 0\n"
        "ds_write_addtid_b32 v196 offset:1088\n"
        "ds_write_addtid_b32 v197 offset:1360\n"
        "v_mfma_f32_16x16x32_f16 v[156:159], v[172:175], v[16:19], 0\n"
        "ds_write_addtid_b32 v198 offset:1632\n"
        "ds_write_addtid_b32 v199 offset:1904\n"
        "s_waitcnt lgkmcnt(7)\n"
        "v_mfma_f32_16x16x32_f16 v[140:143], v[124:127], v[24:27], 0\n"
        "ds_read_b128 v[180:183], v235 offset:1120\n"
        "v_mfma_f32_16x16x32_f16 v[212:215], v[124:127], v[20:23], 0\n"
        "v_mfma_f32_16x16x32_f16 v[216:219], v[124:127], v[12:15], 0\n"
        "s_waitcnt lgkmcnt(1)\n"
        "s_barrier\n"
        "ds_read_b128 v[184:187], v235 offset:1184\n"
        "v_mfma_f32_16x16x32_f16 v[196:199], v[8:11], v[172:175], 0\n"
        "ds_read_b64 v[100:101], v237 offset:384\n"
        "v_mfma_f32_16x16x32_f16 v[200:203], v[56:59], v[176:179], v[200:203]\n"
        "ds_read_b128 v[188:191], v235 offset:1248\n"
        "v_mfma_f32_16x16x32_f16 v[204:207], v[88:91], v[176:179], v[204:207]\n"
        "ds_read_b64 v[102:103], v237 offset:392\n"
        "v_cvt_pk_f16_f32 v136, v136, v208\n"
        "v_cvt_pk_f16_f32 v137, v137, v209\n"
        "v_mfma_f32_16x16x32_f16 v[200:203], v[128:131], v[96:99], v[200:203]\n"
        "s_waitcnt lgkmcnt(4)\n"
        "v_mfma_f32_16x16x32_f16 v[200:203], v[52:55], v[180:183], v[200:203]\n"
        "ds_read_b128 v[168:171], v235 offset:1312\n"
        "v_cvt_pk_f16_f32 v138, v138, v210\n"
        "v_cvt_pk_f16_f32 v139, v139, v211\n"
        "v_mfma_f32_16x16x32_f16 v[204:207], v[84:87], v[180:183], v[204:207]\n"
        "s_waitcnt lgkmcnt(4)\n"
        "v_mfma_f32_16x16x32_f16 v[200:203], v[48:51], v[184:187], v[200:203]\n"
        "ds_read_b128 v[172:175], v235 offset:1376\n"
        "v_cvt_pk_f16_f32 v140, v140, v212\n"
        "v_cvt_pk_f16_f32 v141, v141, v213\n"
        "v_mfma_f32_16x16x32_f16 v[204:207], v[80:83], v[184:187], v[204:207]\n"
        "s_waitcnt lgkmcnt(3)\n"
        "v_mfma_f32_16x16x32_f16 v[200:203], v[44:47], v[188:191], v[200:203]\n"
        "ds_read_b128 v[176:179], v235 offset:1440\n"
        "v_cvt_pk_f16_f32 v142, v142, v214\n"
        "v_cvt_pk_f16_f32 v143, v143, v215\n"
        "v_mfma_f32_16x16x32_f16 v[204:207], v[76:79], v[188:191], v[204:207]\n"
        "s_waitcnt lgkmcnt(3)\n"
        "v_mfma_f32_16x16x32_f16 v[200:203], v[132:135], v[100:103], v[200:203]\n"
        "ds_read_b128 v[180:183], v235 offset:1504\n"
        "v_cvt_pk_f16_f32 v156, v156, v216\n"
        "v_cvt_pk_f16_f32 v157, v157, v217\n"
        "s_waitcnt lgkmcnt(3)\n"
        "v_mfma_f32_16x16x32_f16 v[200:203], v[40:43], v[168:171], v[200:203]\n"
        "v_mfma_f32_16x16x32_f16 v[204:207], v[72:75], v[168:171], v[204:207]\n"
        "v_cvt_pk_f16_f32 v158, v158, v218\n"
        "v_cvt_pk_f16_f32 v159, v159, v219\n"
        "s_waitcnt lgkmcnt(2)\n"
        "v_mfma_f32_16x16x32_f16 v[200:203], v[36:39], v[172:175], v[200:203]\n"
        "v_mfma_f32_16x16x32_f16 v[204:207], v[68:71], v[172:175], v[204:207]\n"
        "s_waitcnt lgkmcnt(1)\n"
        "v_mfma_f32_16x16x32_f16 v[200:203], v[32:35], v[176:179], v[200:203]\n"
        "v_mfma_f32_16x16x32_f16 v[204:207], v[64:67], v[176:179], v[204:207]\n"
        "ds_read_b128 v[124:127], v235 offset:1488\n"
        "s_waitcnt lgkmcnt(1)\n"
        "v_mfma_f32_16x16x32_f16 v[200:203], v[28:31], v[180:183], v[200:203]\n"
        "v_mfma_f32_16x16x32_f16 v[204:207], v[60:63], v[180:183], v[204:207]\n"
        "ds_read_b64 v[104:105], v237 offset:64\n"
        "v_mfma_f32_16x16x32_f16 v[192:195], v[92:95], v[180:183], v[192:195]\n"
        "ds_read_b64 v[106:107], v237 offset:72\n"
        "v_mfma_f32_16x16x32_f16 v[144:147], v[180:183], v[24:27], 0\n"
        "ds_read_b128 v[184:187], v235 offset:1568\n"
        "v_mfma_f32_16x16x32_f16 v[208:211], v[180:183], v[20:23], 0\n"
        "s_nop 0\n"
        "ds_write_addtid_b32 v200 offset:2176\n"
        "ds_write_addtid_b32 v201 offset:2448\n"
        "ds_read_b64 v[108:109], v237 offset:320\n"
        "v_mfma_f32_16x16x32_f16 v[160:163], v[180:183], v[16:19], 0\n"
        "ds_write_addtid_b32 v202 offset:2720\n"
        "ds_write_addtid_b32 v203 offset:2992\n"
        "ds_read_b64 v[110:111], v237 offset:328\n"
        "s_waitcnt lgkmcnt(9)\n"
        "v_mfma_f32_16x16x32_f16 v[148:151], v[124:127], v[24:27], 0\n"
        "ds_read_b128 v[188:191], v235 offset:1632\n"
        "v_mfma_f32_16x16x32_f16 v[212:215], v[124:127], v[20:23], 0\n"
        "v_mfma_f32_16x16x32_f16 v[216:219], v[124:127], v[12:15], 0\n"
        "s_waitcnt lgkmcnt(2)\n"
        "s_barrier\n"
        "v_mfma_f32_16x16x32_f16 v[200:203], v[8:11], v[180:183], 0\n"
        "ds_read_b128 v[168:171], v235 offset:1696\n"
        "v_mfma_f32_16x16x32_f16 v[204:207], v[56:59], v[184:187], v[204:207]\n"
        "v_mfma_f32_16x16x32_f16 v[192:195], v[88:91], v[184:187], v[192:195]\n"
        "ds_read_b128 v[172:175], v235 offset:1760\n"
        "v_cvt_pk_f16_f32 v144, v144, v208\n"
        "v_cvt_pk_f16_f32 v145, v145, v209\n"
        "v_mfma_f32_16x16x32_f16 v[204:207], v[128:131], v[104:107], v[204:207]\n"
        "s_waitcnt lgkmcnt(2)\n"
        "v_mfma_f32_16x16x32_f16 v[204:207], v[52:55], v[188:191], v[204:207]\n"
        "v_cvt_pk_f16_f32 v146, v146, v210\n"
        "v_cvt_pk_f16_f32 v147, v147, v211\n"
        "v_mfma_f32_16x16x32_f16 v[192:195], v[84:87], v[188:191], v[192:195]\n"
        "ds_read_b128 v[176:179], v235 offset:1824\n"
        "v_mfma_f32_16x16x32_f16 v[204:207], v[132:135], v[108:111], v[204:207]\n"
        "v_cvt_pk_f16_f32 v148, v148, v212\n"
        "v_cvt_pk_f16_f32 v149, v149, v213\n"
        "s_waitcnt lgkmcnt(2)\n"
        "v_mfma_f32_16x16x32_f16 v[204:207], v[48:51], v[168:171], v[204:207]\n"
        "ds_read_b128 v[180:183], v235 offset:1888\n"
        "v_mfma_f32_16x16x32_f16 v[192:195], v[80:83], v[168:171], v[192:195]\n"
        "v_cvt_pk_f16_f32 v150, v150, v214\n"
        "v_cvt_pk_f16_f32 v151, v151, v215\n"
        "s_waitcnt lgkmcnt(2)\n"
        "v_mfma_f32_16x16x32_f16 v[204:207], v[44:47], v[172:175], v[204:207]\n"
        "v_mfma_f32_16x16x32_f16 v[192:195], v[76:79], v[172:175], v[192:195]\n"
        "ds_read_b128 v[184:187], v235 offset:1952\n"
        "v_cvt_pk_f16_f32 v160, v160, v216\n"
        "v_cvt_pk_f16_f32 v161, v161, v217\n"
        "v_mfma_f32_16x16x32_f16 v[204:207], v[136:139], v[96:99], v[204:207]\n"
        "ds_read_b64 v[112:113], v237 offset:0\n"
        "s_waitcnt lgkmcnt(3)\n"
        "v_mfma_f32_16x16x32_f16 v[204:207], v[40:43], v[176:179], v[204:207]\n"
        "ds_read_b128 v[188:191], v235 offset:2016\n"
        "v_cvt_pk_f16_f32 v162, v162, v218\n"
        "v_cvt_pk_f16_f32 v163, v163, v219\n"
        "v_mfma_f32_16x16x32_f16 v[192:195], v[72:75], v[176:179], v[192:195]\n"
        "ds_read_b64 v[114:115], v237 offset:8\n"
        "s_waitcnt lgkmcnt(4)\n"
        "v_mfma_f32_16x16x32_f16 v[204:207], v[36:39], v[180:183], v[204:207]\n"
        "ds_read_b64 v[116:117], v237 offset:256\n"
        "v_mfma_f32_16x16x32_f16 v[192:195], v[68:71], v[180:183], v[192:195]\n"
        "ds_read_b64 v[118:119], v237 offset:264\n"
        "v_mfma_f32_16x16x32_f16 v[204:207], v[140:143], v[100:103], v[204:207]\n"
        "s_waitcnt lgkmcnt(5)\n"
        "v_mfma_f32_16x16x32_f16 v[204:207], v[32:35], v[184:187], v[204:207]\n"
        "v_mfma_f32_16x16x32_f16 v[192:195], v[64:67], v[184:187], v[192:195]\n"
        "s_waitcnt lgkmcnt(3)\n"
        "v_mfma_f32_16x16x32_f16 v[204:207], v[28:31], v[188:191], v[204:207]\n"
        "v_mfma_f32_16x16x32_f16 v[192:195], v[60:63], v[188:191], v[192:195]\n"
        "ds_read_b128 v[124:127], v235 offset:2000\n"
        "s_waitcnt lgkmcnt(3)\n"
        "v_mfma_f32_16x16x32_f16 v[192:195], v[128:131], v[112:115], v[192:195]\n"
        "s_waitcnt lgkmcnt(1)\n"
        "v_mfma_f32_16x16x32_f16 v[192:195], v[132:135], v[116:119], v[192:195]\n"
        "v_mfma_f32_16x16x32_f16 v[196:199], v[92:95], v[188:191], v[196:199]\n"
        "s_nop 0\n"
        "ds_write_addtid_b32 v204 offset:3264\n"
        "ds_write_addtid_b32 v205 offset:3536\n"
        "v_mfma_f32_16x16x32_f16 v[128:131], v[188:191], v[24:27], 0\n"
        "ds_write_addtid_b32 v206 offset:3808\n"
        "ds_write_addtid_b32 v207 offset:4080\n"
        "ds_read_b128 v[168:171], v235 offset:2080\n"
        "v_mfma_f32_16x16x32_f16 v[208:211], v[188:191], v[20:23], 0\n"
        "v_mfma_f32_16x16x32_f16 v[164:167], v[188:191], v[16:19], 0\n"
        "s_waitcnt lgkmcnt(5)\n"
        "v_mfma_f32_16x16x32_f16 v[132:135], v[124:127], v[24:27], 0\n"
        "s_waitcnt lgkmcnt(1)\n"
        "s_barrier\n"
        "ds_read_b128 v[172:175], v235 offset:2144\n"
        "v_mfma_f32_16x16x32_f16 v[212:215], v[124:127], v[20:23], 0\n"
        "v_mfma_f32_16x16x32_f16 v[216:219], v[124:127], v[12:15], 0\n"
        "v_mfma_f32_16x16x32_f16 v[204:207], v[8:11], v[188:191], 0\n"
        "ds_read_b128 v[176:179], v235 offset:2208\n"
        "s_waitcnt lgkmcnt(2)\n"
        "v_mfma_f32_16x16x32_f16 v[192:195], v[56:59], v[168:171], v[192:195]\n"
        "v_mfma_f32_16x16x32_f16 v[196:199], v[88:91], v[168:171], v[196:199]\n"
        "v_cvt_pk_f16_f32 v128, v128, v208\n"
        "v_cvt_pk_f16_f32 v129, v129, v209\n"
        "v_mfma_f32_16x16x32_f16 v[192:195], v[136:139], v[104:107], v[192:195]\n"
        "ds_read_b128 v[180:183], v235 offset:2272\n"
        "s_waitcnt lgkmcnt(2)\n"
        "v_mfma_f32_16x16x32_f16 v[192:195], v[52:55], v[172:175], v[192:195]\n"
        "v_cvt_pk_f16_f32 v130, v130, v210\n"
        "v_cvt_pk_f16_f32 v131, v131, v211\n"
        "v_mfma_f32_16x16x32_f16 v[196:199], v[84:87], v[172:175], v[196:199]\n"
        "ds_read_b128 v[184:187], v235 offset:2336\n"
        "v_mfma_f32_16x16x32_f16 v[192:195], v[140:143], v[108:111], v[192:195]\n"
        "ds_read2_b32 v[120:121], v238 offset0:3 offset1:2\n"
        "v_cvt_pk_f16_f32 v132, v132, v212\n"
        "v_cvt_pk_f16_f32 v133, v133, v213\n"
        "s_waitcnt lgkmcnt(3)\n"
        "v_mfma_f32_16x16x32_f16 v[192:195], v[48:51], v[176:179], v[192:195]\n"
        "ds_read2_b32 v[122:123], v238 offset0:1 offset1:0\n"
        "v_mfma_f32_16x16x32_f16 v[196:199], v[80:83], v[176:179], v[196:199]\n"
        "ds_read_b128 v[188:191], v235 offset:2400\n"
        "v_cvt_pk_f16_f32 v134, v134, v214\n"
        "v_cvt_pk_f16_f32 v135, v135, v215\n"
        "v_mfma_f32_16x16x32_f16 v[192:195], v[144:147], v[96:99], v[192:195]\n"
        "s_waitcnt lgkmcnt(4)\n"
        "v_mfma_f32_16x16x32_f16 v[192:195], v[44:47], v[180:183], v[192:195]\n"
        "v_cvt_pk_f16_f32 v164, v164, v216\n"
        "v_cvt_pk_f16_f32 v165, v165, v217\n"
        "v_mfma_f32_16x16x32_f16 v[196:199], v[76:79], v[180:183], v[196:199]\n"
        "ds_read_b128 v[168:171], v235 offset:2464\n"
        "s_waitcnt lgkmcnt(4)\n"
        "v_mfma_f32_16x16x32_f16 v[192:195], v[40:43], v[184:187], v[192:195]\n"
        "v_cvt_pk_f16_f32 v166, v166, v218\n"
        "v_cvt_pk_f16_f32 v167, v167, v219\n"
        "v_mfma_f32_16x16x32_f16 v[196:199], v[72:75], v[184:187], v[196:199]\n"
        "ds_read_b128 v[172:175], v235 offset:2528\n"
        "v_mfma_f32_16x16x32_f16 v[192:195], v[148:151], v[100:103], v[192:195]\n"
        "s_waitcnt lgkmcnt(2)\n"
        "v_mfma_f32_16x16x32_f16 v[192:195], v[36:39], v[188:191], v[192:195]\n"
        "v_mfma_f32_16x16x32_f16 v[196:199], v[68:71], v[188:191], v[196:199]\n"
        "v_mfma_f32_16x16x32_f16 v[192:195], v[152:155], v[120:123], v[192:195]\n"
        "s_waitcnt lgkmcnt(1)\n"
        "v_mfma_f32_16x16x32_f16 v[192:195], v[32:35], v[168:171], v[192:195]\n"
        "v_mfma_f32_16x16x32_f16 v[196:199], v[64:67], v[168:171], v[196:199]\n"
        "s_waitcnt lgkmcnt(0)\n"
        "v_mfma_f32_16x16x32_f16 v[192:195], v[28:31], v[172:175], v[192:195]\n"
        "ds_read_b128 v[124:127], v235 offset:2512\n"
        "v_mfma_f32_16x16x32_f16 v[196:199], v[60:63], v[172:175], v[196:199]\n"
        "v_mfma_f32_16x16x32_f16 v[196:199], v[136:139], v[112:115], v[196:199]\n"
        "ds_read_b128 v[176:179], v235 offset:2592\n"
        "v_mfma_f32_16x16x32_f16 v[196:199], v[140:143], v[116:119], v[196:199]\n"
        "v_mfma_f32_16x16x32_f16 v[200:203], v[92:95], v[172:175], v[200:203]\n"
        "s_nop 1\n"
        "ds_write_addtid_b32 v192 offset:4352\n"
        "ds_write_addtid_b32 v193 offset:4624\n"
        "v_mfma_f32_16x16x32_f16 v[136:139], v[172:175], v[24:27], 0\n"
        "ds_write_addtid_b32 v194 offset:4896\n"
        "ds_write_addtid_b32 v195 offset:5168\n"
        "ds_read_b128 v[180:183], v235 offset:2656\n"
        "v_mfma_f32_16x16x32_f16 v[208:211], v[172:175], v[20:23], 0\n"
        "s_waitcnt lgkmcnt(6)\n"
        "v_mfma_f32_16x16x32_f16 v[140:143], v[124:127], v[24:27], 0\n"
        "v_mfma_f32_16x16x32_f16 v[212:215], v[124:127], v[20:23], 0\n"
        "s_waitcnt lgkmcnt(1)\n"
        "s_barrier\n"
        "ds_read_b128 v[184:187], v235 offset:2720\n"
        "v_mfma_f32_16x16x32_f16 v[196:199], v[56:59], v[176:179], v[196:199]\n"
        "v_mfma_f32_16x16x32_f16 v[200:203], v[88:91], v[176:179], v[200:203]\n"
        "v_cvt_pk_f16_f32 v136, v136, v208\n"
        "v_cvt_pk_f16_f32 v137, v137, v209\n"
        "v_mfma_f32_16x16x32_f16 v[196:199], v[144:147], v[104:107], v[196:199]\n"
        "ds_read_b128 v[188:191], v235 offset:2784\n"
        "s_waitcnt lgkmcnt(2)\n"
        "v_mfma_f32_16x16x32_f16 v[196:199], v[52:55], v[180:183], v[196:199]\n"
        "v_cvt_pk_f16_f32 v138, v138, v210\n"
        "v_cvt_pk_f16_f32 v139, v139, v211\n"
        "v_mfma_f32_16x16x32_f16 v[200:203], v[84:87], v[180:183], v[200:203]\n"
        "v_mfma_f32_16x16x32_f16 v[196:199], v[148:151], v[108:111], v[196:199]\n"
        "ds_read_b128 v[168:171], v235 offset:2848\n"
        "v_cvt_pk_f16_f32 v140, v140, v212\n"
        "v_cvt_pk_f16_f32 v141, v141, v213\n"
        "s_waitcnt lgkmcnt(2)\n"
        "v_mfma_f32_16x16x32_f16 v[196:199], v[48:51], v[184:187], v[196:199]\n"
        "ds_read2_b32 v[8:9], v238 offset0:19 offset1:18\n"
        "v_mfma_f32_16x16x32_f16 v[200:203], v[80:83], v[184:187], v[200:203]\n"
        "ds_read2_b32 v[10:11], v238 offset0:17 offset1:16\n"
        "v_cvt_pk_f16_f32 v142, v142, v214\n"
        "v_cvt_pk_f16_f32 v143, v143, v215\n"
        "v_mfma_f32_16x16x32_f16 v[196:199], v[128:131], v[96:99], v[196:199]\n"
        "ds_read_b128 v[172:175], v235 offset:2912\n"
        "s_waitcnt lgkmcnt(4)\n"
        "v_mfma_f32_16x16x32_f16 v[196:199], v[44:47], v[188:191], v[196:199]\n"
        "v_mfma_f32_16x16x32_f16 v[200:203], v[76:79], v[188:191], v[200:203]\n"
        "v_mfma_f32_16x16x32_f16 v[196:199], v[132:135], v[100:103], v[196:199]\n"
        "ds_read_b128 v[176:179], v235 offset:2976\n"
        "s_waitcnt lgkmcnt(4)\n"
        "v_mfma_f32_16x16x32_f16 v[196:199], v[40:43], v[168:171], v[196:199]\n"
        "v_mfma_f32_16x16x32_f16 v[200:203], v[72:75], v[168:171], v[200:203]\n"
        "ds_read_b128 v[180:183], v235 offset:3040\n"
        "v_mfma_f32_16x16x32_f16 v[196:199], v[156:159], v[120:123], v[196:199]\n"
        "s_waitcnt lgkmcnt(2)\n"
        "v_mfma_f32_16x16x32_f16 v[196:199], v[36:39], v[172:175], v[196:199]\n"
        "v_mfma_f32_16x16x32_f16 v[200:203], v[68:71], v[172:175], v[200:203]\n"
        "v_mfma_f32_16x16x32_f16 v[196:199], v[152:155], v[8:11], v[196:199]\n"
        "s_waitcnt lgkmcnt(1)\n"
        "v_mfma_f32_16x16x32_f16 v[196:199], v[32:35], v[176:179], v[196:199]\n"
        "v_mfma_f32_16x16x32_f16 v[200:203], v[64:67], v[176:179], v[200:203]\n"
        "s_waitcnt lgkmcnt(0)\n"
        "v_mfma_f32_16x16x32_f16 v[196:199], v[28:31], v[180:183], v[196:199]\n"
        "ds_read_b128 v[124:127], v235 offset:3024\n"
        "v_mfma_f32_16x16x32_f16 v[200:203], v[60:63], v[180:183], v[200:203]\n"
        "v_mfma_f32_16x16x32_f16 v[200:203], v[144:147], v[112:115], v[200:203]\n"
        "ds_read_b128 v[184:187], v235 offset:3104\n"
        "v_mfma_f32_16x16x32_f16 v[200:203], v[148:151], v[116:119], v[200:203]\n"
        "v_mfma_f32_16x16x32_f16 v[204:207], v[92:95], v[180:183], v[204:207]\n"
        "s_nop 1\n"
        "ds_write_addtid_b32 v196 offset:5440\n"
        "ds_write_addtid_b32 v197 offset:5712\n"
        "v_mfma_f32_16x16x32_f16 v[144:147], v[180:183], v[24:27], 0\n"
        "ds_write_addtid_b32 v198 offset:5984\n"
        "ds_write_addtid_b32 v199 offset:6256\n"
        "ds_read_b128 v[188:191], v235 offset:3168\n"
        "v_mfma_f32_16x16x32_f16 v[208:211], v[180:183], v[20:23], 0\n"
        "s_waitcnt lgkmcnt(6)\n"
        "v_mfma_f32_16x16x32_f16 v[148:151], v[124:127], v[24:27], 0\n"
        "v_mfma_f32_16x16x32_f16 v[212:215], v[124:127], v[20:23], 0\n"
        "s_waitcnt lgkmcnt(1)\n"
        "s_barrier\n"
        "s_cmp_eq_u32 s30, 0\n"
        "s_cbranch_scc0 .Ls4d_L_MP\n"
        "global_load_dwordx2 v[220:221], v240, s[50:51] sc1\n"
        "global_load_dwordx2 v[222:223], v240, s[52:53] sc1\n"
        "global_load_dwordx2 v[224:225], v240, s[54:55] sc1\n"
        "global_load_dwordx2 v[226:227], v240, s[56:57] sc1\n"
        "global_load_dwordx2 v[228:229], v240, s[58:59] sc1\n"
        "global_load_dwordx2 v[230:231], v241, s[50:51] sc1\n"
        "global_load_dwordx2 v[232:233], v242, s[28:29] sc1\n"
        ".Ls4d_L_MP:\n"
        "ds_read_b128 v[168:171], v235 offset:3232\n"
        "v_mfma_f32_16x16x32_f16 v[200:203], v[56:59], v[184:187], v[200:203]\n"
        "v_mfma_f32_16x16x32_f16 v[204:207], v[88:91], v[184:187], v[204:207]\n"
        "v_cvt_pk_f16_f32 v144, v144, v208\n"
        "v_cvt_pk_f16_f32 v145, v145, v209\n"
        "v_mfma_f32_16x16x32_f16 v[200:203], v[128:131], v[104:107], v[200:203]\n"
        "ds_read_b128 v[172:175], v235 offset:3296\n"
        "s_waitcnt lgkmcnt(2)\n"
        "v_mfma_f32_16x16x32_f16 v[200:203], v[52:55], v[188:191], v[200:203]\n"
        "v_cvt_pk_f16_f32 v146, v146, v210\n"
        "v_cvt_pk_f16_f32 v147, v147, v211\n"
        "v_mfma_f32_16x16x32_f16 v[204:207], v[84:87], v[188:191], v[204:207]\n"
        "v_mfma_f32_16x16x32_f16 v[200:203], v[132:135], v[108:111], v[200:203]\n"
        "ds_read_b128 v[176:179], v235 offset:3360\n"
        "v_cvt_pk_f16_f32 v148, v148, v212\n"
        "v_cvt_pk_f16_f32 v149, v149, v213\n"
        "s_waitcnt lgkmcnt(2)\n"
        "v_mfma_f32_16x16x32_f16 v[200:203], v[48:51], v[168:171], v[200:203]\n"
        "v_mfma_f32_16x16x32_f16 v[204:207], v[80:83], v[168:171], v[204:207]\n"
        "v_cvt_pk_f16_f32 v150, v150, v214\n"
        "v_cvt_pk_f16_f32 v151, v151, v215\n"
        "v_mfma_f32_16x16x32_f16 v[200:203], v[136:139], v[96:99], v[200:203]\n"
        "ds_read_b128 v[180:183], v235 offset:3424\n"
        "s_waitcnt lgkmcnt(2)\n"
        "v_mfma_f32_16x16x32_f16 v[200:203], v[44:47], v[172:175], v[200:203]\n"
        "ds_read2_b32 v[12:13], v238 offset0:35 offset1:34\n"
        "v_mfma_f32_16x16x32_f16 v[204:207], v[76:79], v[172:175], v[204:207]\n"
        "ds_read2_b32 v[14:15], v238 offset0:33 offset1:32\n"
        "v_mfma_f32_16x16x32_f16 v[200:203], v[140:143], v[100:103], v[200:203]\n"
        "ds_read_b128 v[184:187], v235 offset:3488\n"
        "s_waitcnt lgkmcnt(4)\n"
        "v_mfma_f32_16x16x32_f16 v[200:203], v[40:43], v[176:179], v[200:203]\n"
        "v_mfma_f32_16x16x32_f16 v[204:207], v[72:75], v[176:179], v[204:207]\n"
        "v_mfma_f32_16x16x32_f16 v[200:203], v[160:163], v[120:123], v[200:203]\n"
        "ds_read_b128 v[188:191], v235 offset:3552\n"
        "s_waitcnt lgkmcnt(4)\n"
        "v_mfma_f32_16x16x32_f16 v[200:203], v[36:39], v[180:183], v[200:203]\n"
        "v_mfma_f32_16x16x32_f16 v[204:207], v[68:71], v[180:183], v[204:207]\n"
        "v_mfma_f32_16x16x32_f16 v[200:203], v[156:159], v[8:11], v[200:203]\n"
        "s_waitcnt lgkmcnt(1)\n"
        "v_mfma_f32_16x16x32_f16 v[200:203], v[32:35], v[184:187], v[200:203]\n"
        "ds_read_b128 v[168:171], v235 offset:3616\n"
        "v_mfma_f32_16x16x32_f16 v[204:207], v[64:67], v[184:187], v[204:207]\n"
        "v_mfma_f32_16x16x32_f16 v[200:203], v[152:155], v[12:15], v[200:203]\n"
        "s_waitcnt lgkmcnt(1)\n"
        "v_mfma_f32_16x16x32_f16 v[200:203], v[28:31], v[188:191], v[200:203]\n"
        "ds_read_b128 v[172:175], v235 offset:3680\n"
        "v_mfma_f32_16x16x32_f16 v[204:207], v[60:63], v[188:191], v[204:207]\n"
        "v_mfma_f32_16x16x32_f16 v[204:207], v[128:131], v[112:115], v[204:207]\n"
        "ds_read_b128 v[176:179], v235 offset:3744\n"
        "v_mfma_f32_16x16x32_f16 v[204:207], v[132:135], v[116:119], v[204:207]\n"
        "s_waitcnt lgkmcnt(2)\n"
        "v_mfma_f32_16x16x32_f16 v[204:207], v[56:59], v[168:171], v[204:207]\n"
        "s_nop 0\n"
        "ds_write_addtid_b32 v200 offset:6528\n"
        "ds_write_addtid_b32 v201 offset:6800\n"
        "ds_read_b128 v[180:183], v235 offset:3808\n"
        "v_mfma_f32_16x16x32_f16 v[204:207], v[136:139], v[104:107], v[204:207]\n"
        "ds_write_addtid_b32 v202 offset:7072\n"
        "ds_write_addtid_b32 v203 offset:7344\n"
        "v_mfma_f32_16x16x32_f16 v[204:207], v[140:143], v[108:111], v[204:207]\n"
        "ds_read_b128 v[184:187], v235 offset:3872\n"
        "s_waitcnt lgkmcnt(7)\n"
        "v_mfma_f32_16x16x32_f16 v[204:207], v[52:55], v[172:175], v[204:207]\n"
        "ds_read2_b32 v[16:17], v238 offset0:51 offset1:50\n"
        "v_mfma_f32_16x16x32_f16 v[204:207], v[144:147], v[96:99], v[204:207]\n"
        "s_waitcnt lgkmcnt(2)\n"
        "s_barrier\n"
        "ds_read_b128 v[188:191], v235 offset:3936\n"
        "v_mfma_f32_16x16x32_f16 v[204:207], v[48:51], v[176:179], v[204:207]\n"
        "ds_read2_b32 v[18:19], v238 offset0:49 offset1:48\n"
        "v_mfma_f32_16x16x32_f16 v[204:207], v[148:151], v[100:103], v[204:207]\n"
        "ds_read_b128 v[168:171], v235 offset:4000\n"
        "v_mfma_f32_16x16x32_f16 v[204:207], v[44:47], v[180:183], v[204:207]\n"
        "v_mfma_f32_16x16x32_f16 v[204:207], v[164:167], v[120:123], v[204:207]\n"
        "ds_read_b128 v[172:175], v235 offset:4064\n"
        "s_waitcnt lgkmcnt(5)\n"
        "v_mfma_f32_16x16x32_f16 v[204:207], v[40:43], v[184:187], v[204:207]\n"
        "v_mfma_f32_16x16x32_f16 v[204:207], v[160:163], v[8:11], v[204:207]\n"
        "s_waitcnt lgkmcnt(3)\n"
        "v_mfma_f32_16x16x32_f16 v[204:207], v[36:39], v[188:191], v[204:207]\n"
        "v_mfma_f32_16x16x32_f16 v[204:207], v[156:159], v[12:15], v[204:207]\n"
        "s_waitcnt lgkmcnt(1)\n"
        "v_mfma_f32_16x16x32_f16 v[204:207], v[32:35], v[168:171], v[204:207]\n"
        "v_mfma_f32_16x16x32_f16 v[204:207], v[152:155], v[16:19], v[204:207]\n"
        "s_waitcnt lgkmcnt(0)\n"
        "v_mfma_f32_16x16x32_f16 v[204:207], v[28:31], v[172:175], v[204:207]\n"
        "s_nop 7\n"
        "ds_write_addtid_b32 v204 offset:7616\n"
        "ds_write_addtid_b32 v205 offset:7888\n"
        "ds_write_addtid_b32 v206 offset:8160\n"
        "ds_write_addtid_b32 v207 offset:8432\n"
        "s_cmp_eq_u32 s30, 0\n"
        "s_waitcnt lgkmcnt(0)\n"
        "s_barrier\n"
        ".Ls4d_L_ME:\n"
        "s_cbranch_scc1 .Ls4d_L_FIX\n"
        "s_barrier\n"
        "s_endpgm\n"
        ".Ls4d_L_FIX:\n"
        "s_mov_b32 s3, 1048576\n"
        "v_and_b32 v2, 3, v0\n"
        "v_mul_u32_u24 v3, 4608, v2\n"
        "v_mul_u32_u24 v4, 8704, v2\n"
        "v_lshlrev_b32 v5, 2, v2\n"
        "v_add_u32 v3, 0x1b8c0, v3\n"
        "v_add_u32 v5, 0x272d0, v5\n"
        "ds_read_b32 v14, v5\n"
        "v_lshlrev_b32 v7, 4, v2\n"
        "v_lshrrev_b64 v[8:9], v7, s[40:41]\n"
        "v_lshlrev_b32 v8, 16, v8\n"
        "v_and_b32 v2, 7, v0\n"
        "v_lshlrev_b32 v2, 1, v2\n"
        "v_and_b32 v5, 4, v0\n"
        "v_add_u32 v2, 0x272e0, v2\n"
        "v_add_u32 v5, 0x272c0, v5\n"
        ".Ls4d_L_FIX1:\n"
        "s_waitcnt vmcnt(0)\n"
        "v_cmp_ne_u32 vcc, s33, v221\n"
        "v_cmp_ne_u32 s[12:13], s33, v223\n"
        "v_cmp_ne_u32 s[14:15], s33, v225\n"
        "v_cmp_ne_u32 s[16:17], s33, v227\n"
        "v_cmp_ne_u32 s[18:19], s33, v229\n"
        "v_cmp_ne_u32 s[20:21], s33, v231\n"
        "v_cmp_ne_u32 s[22:23], s33, v233\n"
        "s_or_b64 vcc, vcc, s[12:13]\n"
        "s_or_b64 s[14:15], s[14:15], s[16:17]\n"
        "s_or_b64 s[18:19], s[18:19], s[20:21]\n"
        "s_or_b64 vcc, vcc, s[22:23]\n"
        "s_or_b64 s[14:15], s[14:15], s[18:19]\n"
        "s_or_b64 vcc, vcc, s[14:15]\n"
        "s_cbranch_vccz .Ls4d_L_FIX2\n"
        "s_sub_u32 s3, s3, 1\n"
        "s_cbranch_scc1 .Ls4d_L_FIX2\n"
        "global_load_dwordx2 v[220:221], v240, s[50:51] sc1\n"
        "global_load_dwordx2 v[222:223], v240, s[52:53] sc1\n"
        "global_load_dwordx2 v[224:225], v240, s[54:55] sc1\n"
        "global_load_dwordx2 v[226:227], v240, s[56:57] sc1\n"
        "global_load_dwordx2 v[228:229], v240, s[58:59] sc1\n"
        "global_load_dwordx2 v[230:231], v241, s[50:51] sc1\n"
        "global_load_dwordx2 v[232:233], v242, s[28:29] sc1\n"
        "s_branch .Ls4d_L_FIX1\n"
        ".Ls4d_L_FIX2:\n"
        "s_movk_i32 s3, 0x7ff\n"
        "v_bfi_b32 v10, v232, 0, s3\n"
        "v_lshl_add_u32 v3, v10, 1, v3\n"
        "v_bfe_u32 v11, v10, 2, 2\n"
        "v_bfe_u32 v6, v10, 4, 4\n"
        "v_lshl_add_u32 v11, v11, 4, v6\n"
        "v_lshl_add_u32 v4, v11, 2, v4\n"
        "v_and_b32 v6, 3, v10\n"
        "v_mul_u32_u24 v6, 272, v6\n"
        "v_lshrrev_b32 v11, 8, v10\n"
        "v_mul_u32_u24 v11, 1088, v11\n"
        "v_add3_u32 v4, v4, v6, v11\n"
        "ds_read_u16 v3, v3\n"
        "ds_read_b32 v4, v4\n"
        "s_mov_b64 vcc, 0xffffff\n"
        "v_cndmask_b32 v12, 0, v230, vcc\n"
        "v_add_f32 v12, v12, v220\n"
        "v_add_f32 v13, v222, v224\n"
        "v_add_f32 v12, v12, v226\n"
        "v_add_f32 v13, v13, v228\n"
        "v_ashrrev_i32 v15, 31, v232\n"
        "v_add_f32 v12, v12, v13\n"
        "v_not_b32 v15, v15\n"
        "v_or_b32 v15, 0x80000000, v15\n"
        "v_add_f32_dpp v12, v12, v12 row_ror:8 row_mask:0xf bank_mask:0xf bound_ctrl:1\n"
        "v_xor_b32 v15, v15, v232\n"
        "v_mov_b32 v13, v12\n"
        "v_and_b32 v15, 0xffff0000, v15\n"
        "v_mul_f32 v15, 0xbfb8aa3b, v15\n"
        "v_permlane16_swap_b32 v12, v13\n"
        "v_exp_f32 v15, v15\n"
        "v_add_f32 v12, v12, v13\n"
        "v_mov_b32 v13, v12\n"
        "v_add_f32 v15, 1.0, v15\n"
        "v_rcp_f32 v15, v15\n"
        "v_permlane32_swap_b32 v12, v13\n"
        "v_add_f32 v12, v12, v13\n"
        "s_waitcnt lgkmcnt(0)\n"
        "v_cvt_f32_f16 v3, v3\n"
        "v_mul_f32 v4, v4, v14\n"
        "v_mul_f32 v3, 0xbf317218, v3\n"
        "v_mul_f32 v4, 0xbf317218, v4\n"
        "v_cvt_pk_bf16_f32 v12, v12, v15\n"
        "v_cvt_pk_bf16_f32 v3, v3, v4\n"
        "v_lshlrev_b32 v13, 16, v12\n"
        "v_and_b32 v12, 0xffff0000, v12\n"
        "v_and_b32 v4, 0xffff0000, v3\n"
        "v_lshlrev_b32 v3, 16, v3\n"
        "v_mul_f32 v12, v12, v13\n"
        "v_mul_f32 v8, v8, v3\n"
        "v_cvt_pk_bf16_f32 v12, v12, v8\n"
        "v_lshlrev_b32 v13, 16, v12\n"
        "v_and_b32 v12, 0xffff0000, v12\n"
        "v_add_f32 v13, v13, v3\n"
        "v_add_f32 v12, v12, v4\n"
        "v_cvt_pk_bf16_f32 v12, v13, v12\n"
        "v_lshlrev_b32 v13, 16, v12\n"
        "v_mul_f32 v15, 0xbfb8aa3b, v13\n"
        "v_exp_f32 v15, v15\n"
        "v_and_b32 v12, 0xffff0000, v12\n"
        "v_add_f32 v15, 1.0, v15\n"
        "v_rcp_f32 v15, v15\n"
        "v_lshlrev_b32 v6, 16, v230\n"
        "v_mul_f32 v13, v13, v15\n"
        "v_cvt_pk_bf16_f32 v13, v13, v13\n"
        "v_and_b32 v13, 0xffff0000, v13\n"
        "v_mul_f32 v12, v12, v13\n"
        "v_cvt_pk_bf16_f32 v12, v12, v12\n"
        "v_and_b32 v12, 0xffff0000, v12\n"
        "v_add_f32 v12, v12, v6\n"
        "v_cvt_pk_bf16_f32 v12, v12, v12\n"
        "s_mov_b32 exec_lo, 0xff000000\n"
        "s_mov_b32 exec_hi, 0\n"
        "ds_write_b16 v2, v12\n"
        "ds_write_b32 v5, v232\n"
        "s_waitcnt lgkmcnt(0)\n"
        "s_barrier\n"
        "s_endpgm\n"
        ".Ls4d_KEND:\n"
        :
        : "{s[0:1]}"(__builtin_amdgcn_kernarg_segment_ptr()), "{s2}"(blockIdx.x), "{v0}"(threadIdx.x), "s"((__attribute__((address_space(3))) char*)smem)
        : "v1", "v2", "v3", "v4", "v5", "v6", "v7", "v8", "v9", "v10", "v11", "v12", "v13", "v14", "v15", "v16", "v17", "v18", "v19", "v20", "v21", "v22", "v23", "v24", "v25", "v26", "v27", "v28", "v29", "v30", "v31", "v32", "v33", "v34", "v35", "v36", "v37", "v38", "v39", "v40", "v41", "v42", "v43", "v44", "v45", "v46", "v47", "v48", "v49", "v50", "v51", "v52", "v53", "v54", "v55", "v56", "v57", "v58", "v59", "v60", "v61", "v62", "v63", "v64", "v65", "v66", "v67", "v68", "v69", "v70", "v71", "v72", "v73", "v74", "v75", "v76", "v77", "v78", "v79", "v80", "v81", "v82", "v83", "v84", "v85", "v86", "v87", "v88", "v89", "v90", "v91", "v92", "v93", "v94", "v95", "v96", "v97", "v98", "v99", "v100", "v101", "v102", "v103", "v104", "v105", "v106", "v107", "v108", "v109", "v110", "v111", "v112", "v113", "v114", "v115", "v116", "v117", "v118", "v119", "v120", "v121", "v122", "v123", "v124", "v125", "v126", "v127", "v128", "v129", "v130", "v131", "v132", "v133", "v134", "v135", "v136", "v137", "v138", "v139", "v140", "v141", "v142", "v143", "v144", "v145", "v146", "v147", "v148", "v149", "v150", "v151", "v152", "v153", "v154", "v155", "v156", "v157", "v158", "v159", "v160", "v161", "v162", "v163", "v164", "v165", "v166", "v167", "v168", "v169", "v170", "v171", "v172", "v173", "v174", "v175", "v176", "v177", "v178", "v179", "v180", "v181", "v182", "v183", "v184", "v185", "v186", "v187", "v188", "v189", "v190", "v191", "v192", "v193", "v194", "v195", "v196", "v197", "v198", "v199", "v200", "v201", "v202", "v203", "v204", "v205", "v206", "v207", "v208", "v209", "v210", "v211", "v212", "v213", "v214", "v215", "v216", "v217", "v218", "v219", "v220", "v221", "v222", "v223", "v224", "v225", "v226", "v227", "v228", "v229", "v230", "v231", "v232", "v233", "v234", "v235", "v236", "v237", "v238", "v239", "v240", "v241", "v242", "v243", "v244", "v245", "v246", "v247", "v248", "v249", "v250", "v251", "v252", "v253", "v254", "v255", "s3", "s4", "s5", "s6", "s7", "s8", "s9", "s10", "s11", "s12", "s13", "s14", "s15", "s16", "s17", "s18", "s19", "s20", "s21", "s22", "s23", "s24", "s25", "s26", "s27", "s28", "s29", "s30", "s31", "s33", "s34", "s35", "s36", "s37", "s38", "s39", "s40", "s41", "s42", "s43", "s45", "s46", "s47", "s48", "s49", "s50", "s51", "s52", "s53", "s54", "s55", "s56", "s57", "s58", "s59", "s60", "s61", "s62", "s63", "vcc", "scc", "memory");
}

extern "C" void kernel_launch(void* const* d_in, const int* in_sizes, int n_in,
                              void* d_out, int out_size, void* d_ws, size_t ws_size,
                              hipStream_t stream) {
    s4d_mod_kernel<<<256, 512, 0, stream>>>(d_in[0], d_in[1], d_in[2], d_in[3], d_in[4], d_in[5], d_in[6], d_in[8], d_in[9], d_in[10], d_in[11], d_out, d_ws);
}
```
